```python
import torch

def _softmax(x, axis=-1):
    return torch.softmax(x, dim=axis)

def _gelu(x):
    return torch.nn.functional.gelu(x, approximate='tanh')

def _silu(x):
    return torch.nn.functional.silu(x)

def _sigmoid(x):
    return torch.sigmoid(x)

def _layernorm(x, axis=-1, eps=1e-05):
    m = x.mean(dim=axis, keepdim=True)
    v = x.var(dim=axis, keepdim=True, unbiased=False)
    return (x - m) / torch.sqrt(v + eps)

def _scatter_add(tgt, idx, src, axis=0):
    return tgt.clone().index_add_(axis, idx, src)

def _segment_sum(src, seg, n):
    out = torch.zeros((n,) + src.shape[1:], dtype=src.dtype)
    return out.index_add_(0, seg, src)

def _pad(x, pw):
    flat = tuple((v for pair in reversed(pw) for v in pair))
    return torch.nn.functional.pad(x, flat)
kernel_name = 'adaLN_cond__gqa_block_cached__dense_activation_silu__decode'

def setup_inputs():

    _w = lambda a: (lambda t: t.to(torch.bfloat16) if t.is_floating_point() else t)(torch.as_tensor(a))

    def _A_setup():
        B, Sq, D = (1, 1, 576)
        Hq, Hkv, Sk, Dh = (32, 16, 8192, 128)
        assert Hq % Hkv == 0, (Hq, Hkv)
        return {'x': _w((lambda *s: torch.randn(*s, dtype=torch.bfloat16) * 0.1)(B, Sq, D)), 'k_cache': _w((lambda *s: torch.randn(*s, dtype=torch.bfloat16) * 1.6)(B, Hkv, Sk, Dh)), 'v_cache': _w((lambda *s: torch.randn(*s, dtype=torch.bfloat16) * 3.2)(B, Hkv, Sk, Dh)), 'wq': _w((lambda *s: torch.randn(*s, dtype=torch.bfloat16) * 1.6)(D, Hq * Dh) * D ** (-0.5)), 'wo': _w((lambda *s: torch.randn(*s, dtype=torch.bfloat16) * 3.2)(Hq * Dh, D) * (Hq * Dh) ** (-0.5))}
    _A_in = _A_setup()

    def _B_setup():
        B, S, D, F = (1, 1, 576, 4608)
        return {'x': _w((lambda *s: torch.randn(*s, dtype=torch.bfloat16) * 0.1)(B, S, D)), 'w1': _w((lambda *s: torch.randn(*s, dtype=torch.bfloat16) * 3.2)(D, F) * D ** (-0.5)), 'w2': _w((lambda *s: torch.randn(*s, dtype=torch.bfloat16) * 3.2)(F, D) * F ** (-0.5))}
    _B_in = _B_setup()
    return {'A_x': _A_in['x'], 'A_k_cache': _A_in['k_cache'], 'A_v_cache': _A_in['v_cache'], 'A_wq': _A_in['wq'], 'A_wo': _A_in['wo'], 'B_x': _B_in['x'], 'B_w1': _B_in['w1'], 'B_w2': _B_in['w2'], 'c': _w((lambda *s: torch.randn(*s, dtype=torch.bfloat16) * 0.2)(1, 576) * 8.0), 'w_mod': _w((lambda *s: torch.randn(*s, dtype=torch.bfloat16) * 0.2)(576, 6 * 576) * 576 ** (-0.5) * 8.0)}

def reference(A_x, A_k_cache, A_v_cache, A_wq, A_wo, B_x, B_w1, B_w2, c, w_mod):

    def _A_ref(x, k_cache, v_cache, wq, wo):
        B, Sq, D = x.shape
        Hq, Dh = (32, 128)
        Hkv = k_cache.shape[1]
        rep = Hq // Hkv
        q = (lambda x, *d: x.permute(*d))((x @ wq).reshape(B, Sq, Hq, Dh), 0, 2, 1, 3)
        k = (lambda x, n, axis: torch.repeat_interleave(x, n, dim=axis))(k_cache, rep, axis=1)
        v = (lambda x, n, axis: torch.repeat_interleave(x, n, dim=axis))(v_cache, rep, axis=1)
        scores = torch.einsum('bhqd,bhkd->bhqk', q, k) * Dh ** (-0.5)
        p = _softmax(scores, axis=-1)
        o = torch.einsum('bhqk,bhkd->bhqd', p, v)
        return (lambda x, *d: x.permute(*d))(o, 0, 2, 1, 3).reshape(B, Sq, Hq * Dh) @ wo

    def _B_ref(x, w1, w2):
        h = torch.einsum('bsd,df->bsf', x, w1)
        h = _silu(h)
        out = torch.einsum('bsf,fd->bsd', h, w2)
        return out
    x = A_x
    mod = (_silu(c) @ w_mod).reshape(c.shape[0], 6, 1, -1)
    sa, ca, ga, sb, cb, gb = (mod[:, 0].clone(), mod[:, 1].clone(), mod[:, 2].clone(), mod[:, 3].clone(), mod[:, 4].clone(), mod[:, 5].clone())
    h = _layernorm(x, axis=-1) * (1 + ca) + sa
    x = x + ga * _A_ref(h, A_k_cache, A_v_cache, A_wq, A_wo)
    h = _layernorm(x, axis=-1) * (1 + cb) + sb
    x = x + gb * _B_ref(h, B_w1, B_w2)
    return x
```

```cpp
#include <hip/hip_runtime.h>
#include <hip/hip_bf16.h>
#include <stdint.h>

typedef unsigned short u16;
typedef unsigned u32x4 __attribute__((ext_vector_type(4)));
constexpr int D = 576, HQ = 32, HKV = 16, SK = 8192, DH = 128, F = 4608;
constexpr int NSPLIT = 16;

__device__ __forceinline__ float bf2f(u16 b) { return __uint_as_float(((unsigned)b) << 16); }
__device__ __forceinline__ float rbf(float f) { return __bfloat162float(__float2bfloat16(f)); }

template <int MODE>
__global__ void gemv_k(const float* __restrict__ x, const u16* __restrict__ W, float* __restrict__ out, int K, int N) {
  __shared__ float red[8][64];
  int n = blockIdx.x * 64 + (threadIdx.x & 63);
  int w = threadIdx.x >> 6;
  float acc = 0.f;
  for (int k = w; k < K; k += 8) acc += x[k] * bf2f(W[(size_t)k * N + n]);
  red[w][threadIdx.x & 63] = acc;
  __syncthreads();
  if (w == 0) {
    float s = 0.f;
    for (int i = 0; i < 8; ++i) s += red[i][threadIdx.x];
    s = rbf(s);
    if (MODE == 1) { s = rbf(s / (1.f + __expf(-s))); }
    out[n] = s;
  }
}

__global__ void silu_c(const u16* c, float* out) {
  int i = threadIdx.x; if (i < D) { float v = bf2f(c[i]); out[i] = rbf(v / (1.f + __expf(-v))); }
}

__global__ void ln_mod(const float* xin_f, const u16* xin_b, const float* g, const float* y, const float* cc, const float* ss, float* xout, float* h, u16* outb) {
  __shared__ float sh[1024];
  int i = threadIdx.x;
  float x = 0.f;
  if (i < D) {
    x = xin_b ? bf2f(xin_b[i]) : xin_f[i];
    if (g) x = rbf(x + rbf(g[i] * y[i]));
    if (xout) xout[i] = x;
    if (outb) { __hip_bfloat16 b = __float2bfloat16(x); outb[i] = *(u16*)&b; }
  }
  if (!h) return;
  sh[i] = x; __syncthreads();
  for (int s = 512; s > 0; s >>= 1) { if (i < s) sh[i] += sh[i + s]; __syncthreads(); }
  float mean = sh[0] / D; __syncthreads();
  float dv = (i < D) ? (x - mean) : 0.f;
  sh[i] = dv * dv; __syncthreads();
  for (int s = 512; s > 0; s >>= 1) { if (i < s) sh[i] += sh[i + s]; __syncthreads(); }
  float var = rbf(sh[0] / D);
  float m = rbf(mean);
  if (i < D) {
    float ln = rbf(rbf(x - m) / rbf(sqrtf(rbf(var + 1e-5f))));
    h[i] = rbf(rbf(ln * rbf(1.f + cc[i])) + ss[i]);
  }
}

__global__ void attn_split(const float* q, const u16* K, const u16* V, float* pm, float* pl, float* po) {
  __shared__ float sq[2][DH];
  __shared__ float sc[2][SK / NSPLIT];
  __shared__ float red[256];
  int s = blockIdx.x, kvh = blockIdx.y, t = threadIdx.x;
  constexpr int NK = SK / NSPLIT;
  sq[t >> 7][t & 127] = q[kvh * 256 + t];
  __syncthreads();
  const u16* Kb = K + ((size_t)kvh * SK + s * NK) * DH;
  const u16* Vb = V + ((size_t)kvh * SK + s * NK) * DH;
  for (int k = t; k < NK; k += 256) {
    float a0 = 0, a1 = 0;
    for (int d = 0; d < DH; ++d) { float kv = bf2f(Kb[(size_t)k * DH + d]); a0 += kv * sq[0][d]; a1 += kv * sq[1][d]; }
    sc[0][k] = rbf(rbf(a0) * 0.08838834764831845f);
    sc[1][k] = rbf(rbf(a1) * 0.08838834764831845f);
  }
  __syncthreads();
  int h = t >> 7, d = t & 127;
  float m = -INFINITY;
  for (int k = 0; k < NK; ++k) m = fmaxf(m, sc[h][k]);
  float l = 0, acc = 0;
  for (int k = 0; k < NK; ++k) { float p = __expf(sc[h][k] - m); l += p; acc += p * bf2f(Vb[(size_t)k * DH + d]); }
  int head = kvh * 2 + h;
  po[((size_t)head * NSPLIT + s) * DH + d] = acc;
  if (d == 0) { pm[head * NSPLIT + s] = m; pl[head * NSPLIT + s] = l; }
}

__global__ void attn_comb(const float* pm, const float* pl, const float* po, float* o) {
  int head = blockIdx.x, d = threadIdx.x;
  float M = -INFINITY;
  for (int s = 0; s < NSPLIT; ++s) M = fmaxf(M, pm[head * NSPLIT + s]);
  float L = 0, a = 0;
  for (int s = 0; s < NSPLIT; ++s) { float w = __expf(pm[head * NSPLIT + s] - M); L += w * pl[head * NSPLIT + s]; a += w * po[((size_t)head * NSPLIT + s) * DH + d]; }
  o[head * DH + d] = rbf(a / L);
}

extern "C" void kernel_launch(void* const* d_in, const int* in_sizes, int n_in, void* d_out, int out_size, void* d_ws, size_t ws_size, hipStream_t stream) {
  const u16* x = (const u16*)d_in[0];
  const u16* K = (const u16*)d_in[1];
  const u16* V = (const u16*)d_in[2];
  const u16* wq = (const u16*)d_in[3];
  const u16* wo = (const u16*)d_in[4];
  const u16* w1 = (const u16*)d_in[6];
  const u16* w2 = (const u16*)d_in[7];
  const u16* c = (const u16*)d_in[8];
  const u16* wm = (const u16*)d_in[9];
  float* ws = (float*)d_ws;
  float* sc = ws; float* mod = ws + 1024; float* h1 = ws + 8192; float* q = ws + 9216; float* pm = ws + 16384; float* pl = ws + 17408;
  float* o = ws + 20480; float* ao = ws + 24576; float* x1 = ws + 25600; float* h2 = ws + 26624; float* a = ws + 28672; float* y2 = ws + 36864; float* po = ws + 65536;
  silu_c<<<1, 1024, 0, stream>>>(c, sc);
  gemv_k<0><<<3456 / 64, 512, 0, stream>>>(sc, wm, mod, D, 3456);
  ln_mod<<<1, 1024, 0, stream>>>(nullptr, x, nullptr, nullptr, mod + 576, mod, nullptr, h1, nullptr);
  gemv_k<0><<<4096 / 64, 512, 0, stream>>>(h1, wq, q, D, 4096);
  attn_split<<<dim3(NSPLIT, HKV), 256, 0, stream>>>(q, K, V, pm, pl, po);
  attn_comb<<<32, 128, 0, stream>>>(pm, pl, po, o);
  gemv_k<0><<<576 / 64, 512, 0, stream>>>(o, wo, ao, 4096, D);
  ln_mod<<<1, 1024, 0, stream>>>(nullptr, x, mod + 2 * 576, ao, mod + 4 * 576, mod + 3 * 576, x1, h2, nullptr);
  gemv_k<1><<<4608 / 64, 512, 0, stream>>>(h2, w1, a, D, F);
  gemv_k<0><<<576 / 64, 512, 0, stream>>>(a, w2, y2, F, D);
  ln_mod<<<1, 1024, 0, stream>>>(x1, nullptr, mod + 5 * 576, y2, nullptr, nullptr, nullptr, nullptr, (u16*)d_out);
}
```

```cpp
#include <hip/hip_runtime.h>
#include <stdint.h>

typedef unsigned u32;
typedef unsigned long long u64;
typedef unsigned u32x4 __attribute__((ext_vector_type(4)));
typedef unsigned u32x2 __attribute__((ext_vector_type(2)));
typedef float f32x4 __attribute__((ext_vector_type(4)));
typedef __bf16 bf16x8 __attribute__((ext_vector_type(8)));
typedef __bf16 bf16x2 __attribute__((ext_vector_type(2)));
typedef __amdgpu_buffer_rsrc_t rsrc_t;

#define KV_AUX 2
#define W_AUX 2
#define WIN 3
#define NEXTRA 2
#define NTEARLY 3
#define WIN0 3
#define POLL2 0
#define NTHR 24

constexpr int D = 576, SK = 8192, FF = 4608, NMOD = 3456, NQ = 4096;
constexpr int SC1 = 16;
constexpr u32 SENT = 0xFFFFFFFFu;
constexpr int SPIN_CAP = 1 << 17;

constexpr int WS_EPOCH = 0;
constexpr int WS_MODP = 4096;
constexpr int WS_MODF = 4096 + 110592;
constexpr int WS_QP = 131072;
constexpr int WS_CLR = 262144;
constexpr int WS_OP = WS_CLR;
constexpr int WS_ML = WS_OP + 262144;
constexpr int WS_WP = WS_ML + 32768;
constexpr int WS_YP = WS_WP + 18432;
constexpr int WS_CLR_END = WS_YP + 72 * 2304;
constexpr int YCLR = 656;
static_assert(256 * YCLR >= 72 * 2304, "clear covers the region");
constexpr int WS_STAMP = 1 << 20;
constexpr int WS_BYTES = 2 << 20;
constexpr int OOB = 0x7FFFFFF0;

constexpr int L_Q = 0;
constexpr int L_MAX = 512;
constexpr int L_SUM = 576;
constexpr int L_STAT = 640;
constexpr int L_ORED = 768;
constexpr int L_RED = L_ORED + 8192;
constexpr int L_MODV = L_RED + 4608;
constexpr int L_WGT = L_MODV + 3584;
constexpr int L_ZERO = L_WGT + 256;
constexpr int L_BYTES = L_ZERO + 256;

__device__ __forceinline__ rsrc_t mk(const void* p, u32 bytes) { return __builtin_amdgcn_make_buffer_rsrc((void*)p, 0, bytes, 0x00020000); }
template <int AUX> __device__ __forceinline__ u32x4 ld128(rsrc_t r, int v, int s) { return __builtin_amdgcn_raw_buffer_load_b128(r, v, s, AUX); }
template <int AUX> __device__ __forceinline__ u32x2 ld64(rsrc_t r, int v, int s) { return __builtin_amdgcn_raw_buffer_load_b64(r, v, s, AUX); }
template <int AUX> __device__ __forceinline__ u32 ld32(rsrc_t r, int v, int s) { return __builtin_amdgcn_raw_buffer_load_b32(r, v, s, AUX); }
__device__ __forceinline__ void st128(rsrc_t r, int v, u32x4 x) { __builtin_amdgcn_raw_buffer_store_b128(x, r, v, 0, SC1); }
__device__ __forceinline__ float asf(u32 x) { return __builtin_bit_cast(float, x); }
__device__ __forceinline__ u32 asu(float x) { return __builtin_bit_cast(u32, x); }
__device__ __forceinline__ u32 pk2(float a, float b) { bf16x2 v = {(__bf16)a, (__bf16)b}; return __builtin_bit_cast(u32, v); }
__device__ __forceinline__ float rbf(float a) { return (float)(__bf16)a; }
__device__ __forceinline__ float lo16(u32 x) { return asf(x << 16); }
__device__ __forceinline__ float hi16(u32 x) { return asf(x & 0xFFFF0000u); }
template <int C> __device__ __forceinline__ u32 dppu(u32 x) { return (u32)__builtin_amdgcn_mov_dpp((int)x, C, 0xf, 0xf, true); }
template <int C> __device__ __forceinline__ float dppf(float x) { return asf(dppu<C>(asu(x))); }
constexpr int XOR1 = 0xB1, XOR2 = 0x4E, HMIR = 0x141, ROR8 = 0x128, ROR4 = 0x124;
__device__ __forceinline__ float fexp(float x) { return __builtin_amdgcn_exp2f(x * 1.4426950408889634f); }
__device__ __forceinline__ float silu(float x) { return x * __builtin_amdgcn_rcpf(1.f + fexp(-x)); }
__device__ __forceinline__ f32x4 mfma(u32x4 a, u32x4 b, f32x4 c) {
  return __builtin_amdgcn_mfma_f32_16x16x32_bf16(__builtin_bit_cast(bf16x8, a), __builtin_bit_cast(bf16x8, b), c, 0, 0, 0);
}
__device__ __forceinline__ float x16_sum(float x) {
  auto s = __builtin_amdgcn_permlane16_swap(asu(x), asu(x), false, false);
  x = asf(s[0]) + asf(s[1]);
  auto t = __builtin_amdgcn_permlane32_swap(asu(x), asu(x), false, false);
  return asf(t[0]) + asf(t[1]);
}
__device__ __forceinline__ float x16_max(float x) {
  auto s = __builtin_amdgcn_permlane16_swap(asu(x), asu(x), false, false);
  x = fmaxf(asf(s[0]), asf(s[1]));
  auto t = __builtin_amdgcn_permlane32_swap(asu(x), asu(x), false, false);
  return fmaxf(asf(t[0]), asf(t[1]));
}
__device__ __forceinline__ float row_sum(float x) {
  x += dppf<XOR1>(x); x += dppf<XOR2>(x); x += dppf<HMIR>(x); x += dppf<ROR8>(x); return x;
}
__device__ __forceinline__ float row_max(float x) {
  x = fmaxf(x, dppf<XOR1>(x)); x = fmaxf(x, dppf<XOR2>(x)); x = fmaxf(x, dppf<HMIR>(x)); x = fmaxf(x, dppf<ROR8>(x)); return x;
}
__device__ __forceinline__ float wave_sum(float x) { return x16_sum(row_sum(x)); }
__device__ __forceinline__ void bar() { asm volatile("s_waitcnt lgkmcnt(0)\n\ts_barrier" ::: "memory"); }
__device__ __forceinline__ void pin() { __builtin_amdgcn_sched_barrier(0); }
__device__ __forceinline__ u32x4 diag(u32 dup, const u32x4& m) { return u32x4{dup & m.x, dup & m.y, dup & m.z, dup & m.w}; }
__device__ __forceinline__ f32x4 fold(f32x4 a) {
  f32x4 r;
#pragma unroll
  for (int i = 0; i < 4; ++i) {
    const u32 t = dppu<ROR8>(asu(a[i]));
    auto s = __builtin_amdgcn_permlane32_swap(t, t, false, false);
    r[i] = a[i] + asf(s[1]);
  }
  return r;
}
__device__ __forceinline__ f32x4 gemv8(u32 dup, const u32x4* w, const u32x4& m, f32x4 a = f32x4{0.f, 0.f, 0.f, 0.f}) {
  const u32 lo = (u32)__builtin_amdgcn_update_dpp((int)dup, (int)dup, 0x114, 0xf, 0xa, false);
  const u32 hi = (u32)__builtin_amdgcn_update_dpp((int)dup, (int)dup, 0x104, 0xf, 0x5, false);
  a = mfma(diag(dppu<0x00>(lo), m), w[0], a);
  a = mfma(diag(dppu<0x55>(lo), m), w[1], a);
  a = mfma(diag(dppu<0xAA>(lo), m), w[2], a);
  a = mfma(diag(dppu<0xFF>(lo), m), w[3], a);
  a = mfma(diag(dppu<0x00>(hi), m), w[4], a);
  a = mfma(diag(dppu<0x55>(hi), m), w[5], a);
  a = mfma(diag(dppu<0xAA>(hi), m), w[6], a);
  a = mfma(diag(dppu<0xFF>(hi), m), w[7], a);
  return a;
}
__device__ __forceinline__ u32 nosent(float x) { return min(asu(x), 0xFFFFFFFEu); }
__device__ __forceinline__ u32x4 nosent4(f32x4 a) { return u32x4{nosent(a[0]), nosent(a[1]), nosent(a[2]), nosent(a[3])}; }
__device__ __forceinline__ bool gran_ok(u32x4 x, u32 tag, u32 t8) { return x.y == tag && x.w == tag && (x.x & 255u) == t8 && (x.z & 255u) == t8; }
__device__ __forceinline__ void pub_gran(rsrc_t ws, int base, f32x4 a, u32 tag, u32 t8, int l) {
  if (l < 32 && (l & 15) < 8) {
    const int o = base + (8 * (l & 7) + 4 * (l >> 4)) * 8;
    st128(ws, o, u32x4{(asu(a[0]) & ~255u) | t8, tag, (asu(a[1]) & ~255u) | t8, tag});
    st128(ws, o + 16, u32x4{(asu(a[2]) & ~255u) | t8, tag, (asu(a[3]) & ~255u) | t8, tag});
  }
}
template <int N> __device__ __forceinline__ bool gran_all(const u32x4 (&v)[N], u32 tag, u32 t8) {
  u32 a = 0, bq = 0;
#pragma unroll
  for (int i = 0; i < N; ++i) { a |= (v[i].y ^ tag) | (v[i].w ^ tag); bq |= (v[i].x ^ t8) | (v[i].z ^ t8); }
  return __all((a | (bq & 255u)) == 0u);
}
template <int N, class F> __device__ __forceinline__ void poll_gran(F ld, u32x4 (&v)[N], u32 tag, u32 t8) {
  _Pragma("unroll 1") for (int spin = 0; spin < SPIN_CAP; ++spin) {
    asm volatile("" ::: "memory");
#pragma unroll
    for (int i = 0; i < N; ++i) v[i] = ld(i);
    if (gran_all<N>(v, tag, t8)) break;
  }
}
__device__ __forceinline__ float gv(u32 x) { return asf(x & ~255u); }
#define STAMP(k) do {} while (0)
#define STAMP_DECL
#define STAMP_DUMP() do {} while (0)

#define END() do { STAMP_DUMP(); __builtin_amdgcn_endpgm(); } while (0)
struct Ctx { const void *xp, *wop, *w1p, *w2p; void *outp, *wsp; unsigned char* lds; rsrc_t ws; int b, l, w, xg, idx, kvh, sp, r8, c8, jm, fidx; u32x4 m;
};
#define CTX_STAMP const int l = c.l, w = c.w; (void)l; (void)w;

template <int ROLE> __device__ __forceinline__ void tail_load(const Ctx& c, int k, u32x4 (&T1)[8], u32x4 (&T2)[8]) {
  if (ROLE == 0 && k < 8) T1[k] = ld128<W_AUX>(mk(c.wop, NQ * D * 2), ((512 * c.xg + 64 * c.w + 8 * c.r8) * D + 64 * c.idx + 8 * c.c8) * 2, k * D * 2);
  if (ROLE == 1 && k < 8) T1[k] = ld128<W_AUX>(mk(c.w1p, D * FF * 2), ((64 * c.w + 8 * c.r8) * FF + 64 * c.jm + 8 * c.c8) * 2, k * FF * 2);
  if (ROLE == 1 && k >= 8) T2[k - 8] = ld128<W_AUX>(mk(c.w2p, D * FF * 2), ((64 * c.jm + 8 * c.r8) * D + 64 * c.w + 8 * c.c8) * 2, (k - 8) * D * 2);
}

__device__ __forceinline__ void tail_wo(const Ctx& c, u32x2 ml, const u32x4 (&T1)[8]) {
  CTX_STAMP
  const int head = 4 * c.xg + (w >> 1);
  _Pragma("unroll 1") for (int spin = 0; spin < SPIN_CAP; ++spin) {
    if (__all(ml.x != SENT && ml.y != SENT)) break;
    asm volatile("" ::: "memory");
    ml = ld64<SC1>(c.ws, WS_ML + ((head >> 1) * 16 + (l & 15)) * 128 + (head & 1) * 8, 0);
  }
  const float M = row_max(asf(ml.x)), e = fexp(asf(ml.x) - M), L = row_sum(e * asf(ml.y));
  const float wgt = e / L;
  float ws_[16];
#pragma unroll
  for (int s = 0; s < 16; ++s) ws_[s] = asf(__builtin_amdgcn_readlane(asu(wgt), s));
  pin();
  STAMP(8);
  u32 v[16];
  _Pragma("unroll 1") for (int spin = 0; spin < SPIN_CAP; ++spin) {
    asm volatile("" ::: "memory");
#pragma unroll
    for (int s = 0; s < 16; ++s) v[s] = ld32<SC1>(c.ws, WS_OP + head * 8192 + (w & 1) * 256 + l * 4, s * 512);
    u32 mxv = 0;
#pragma unroll
    for (int s = 0; s < 16; ++s) mxv = max(mxv, v[s]);
    if (__all(mxv != SENT)) break;
  }
  STAMP(5);
  float o4[4];
#pragma unroll
  for (int i = 0; i < 4; ++i) o4[i] = fmaf(ws_[i], asf(v[i]), fmaf(ws_[i + 4], asf(v[i + 4]), fmaf(ws_[i + 8], asf(v[i + 8]), ws_[i + 12] * asf(v[i + 12]))));
  const float o = (o4[0] + o4[1]) + (o4[2] + o4[3]);
  const f32x4 a = gemv8(pk2(o, o), T1, c.m);
  if (((l >> 5) ^ (l >> 3)) & 1) {} else *(f32x4*)(c.lds + L_RED + (w * 2 + (l >> 5)) * 256 + (2 * c.c8 + ((l >> 4) & 1)) * 16) = a;
  bar();
  STAMP(6);
}

__device__ __forceinline__ void tail_mlp(const Ctx& c, const u32x4 (&T1)[8], const u32x4 (&T2)[8]) {
  CTX_STAMP
  const int col = 64 * w + l;
  const float xin = asf((u32)((const unsigned short*)c.xp)[col] << 16);
  const unsigned short* mv = (const unsigned short*)(c.lds + L_MODV);
  const float ga = asf((u32)mv[col] << 16), sb = asf((u32)mv[D + col] << 16), cb1 = rbf(1.f + asf((u32)mv[2 * D + col] << 16));
  u32 v[8];
  STAMP(8);
  _Pragma("unroll 1") for (int spin = 0; spin < SPIN_CAP; ++spin) {
    asm volatile("" ::: "memory");
#pragma unroll
    for (int s = 0; s < 8; ++s) v[s] = ld32<SC1>(c.ws, WS_WP + col * 4, s * D * 4);
    u32 mxv = 0;
#pragma unroll
    for (int s = 0; s < 8; ++s) mxv = max(mxv, v[s]);
    if (__all(mxv != SENT)) break;
  }
  STAMP(5);
  const float at = ((asf(v[0]) + asf(v[1])) + (asf(v[2]) + asf(v[3]))) + ((asf(v[4]) + asf(v[5])) + (asf(v[6]) + asf(v[7])));
  const float x1 = rbf(xin + rbf(ga * rbf(at)));
  const float s1 = wave_sum(x1), s2 = wave_sum(x1 * x1);
  if (l == 0) *(float2*)(c.lds + L_STAT + w * 8) = make_float2(s1, s2);
  bar();
  float mean, var;
  { const f32x4 t0 = *(const f32x4*)(c.lds + L_STAT), t1 = *(const f32x4*)(c.lds + L_STAT + 16), t2 = *(const f32x4*)(c.lds + L_STAT + 32),
                t3 = *(const f32x4*)(c.lds + L_STAT + 48); const float2 t4 = *(const float2*)(c.lds + L_STAT + 64);
    const f32x4 t = (t0 + t1) + (t2 + t3);
    mean = ((t[0] + t[2]) + t4.x) * (1.f / D);
    var = fmaxf(((t[1] + t[3]) + t4.y) * (1.f / D) - mean * mean, 0.f); }
  const float rsd = __builtin_amdgcn_rcpf(rbf(__builtin_amdgcn_sqrtf(rbf(rbf(var) + 1e-5f))));
  const float ln = rbf(rbf(x1 - rbf(mean)) * rsd);
  const float h = rbf(ln * cb1) + sb;
  const f32x4 a = gemv8(pk2(h, h), T1, c.m);
  if (((l >> 5) ^ (l >> 3)) & 1) {} else *(f32x4*)(c.lds + L_RED + (w * 2 + (l >> 5)) * 256 + (2 * c.c8 + ((l >> 4) & 1)) * 16) = a;
  STAMP(6);
  bar();
  float u[18];
#pragma unroll
  for (int i = 0; i < 18; ++i) u[i] = *(const float*)(c.lds + L_RED + i * 256 + l * 4);
  const float us = (((u[0] + u[1]) + (u[2] + u[3])) + ((u[4] + u[5]) + (u[6] + u[7]))) + (((u[8] + u[9]) + (u[10] + u[11])) + ((u[12] + u[13]) + (u[14] + u[15]))) + (u[16] + u[17]);
  const float act = silu(rbf(us));
  const f32x4 y = fold(gemv8(pk2(act, act), T2, c.m));
  if (l < 32 && (l & 15) < 8) st128(c.ws, WS_YP + (c.jm * D + 64 * w) * 4 + (2 * c.c8 + (l >> 4)) * 16, nosent4(y));
  STAMP(7);
}

__global__ void __launch_bounds__(640, 3)
fused_k(const void* __restrict__ xp, const void* __restrict__ kp, const void* __restrict__ vp, const void* __restrict__ wqp,
        const void* __restrict__ wop, const void* __restrict__ w1p, const void* __restrict__ w2p, const void* __restrict__ cp,
        const void* __restrict__ wmp, void* __restrict__ outp, void* wsp) {
  __shared__ __attribute__((aligned(16))) unsigned char lds[L_BYTES];
  const int b = blockIdx.x, l = threadIdx.x & 63, w = __builtin_amdgcn_readfirstlane(threadIdx.x >> 6);
  const int xg = b & 7, idx = b >> 3, kvh = xg * 2 + (idx & 1), sp = idx >> 1;
  const int r8 = l >> 3, c8 = l & 7;
  const rsrc_t ws = mk(wsp, WS_BYTES);
  const int fidx = b - 144;
  const int role = idx < 9 ? 0 : idx < 18 ? 1 : fidx < 18 ? 2 : 3;
  const int jm = xg * 9 + idx - 9;
  u32x4 m;
  { const u32 h = (l & 1) ? 0xFFFF0000u : 0x0000FFFFu; const int d = c8 >> 1;
    m = u32x4{d == 0 ? h : 0u, d == 1 ? h : 0u, d == 2 ? h : 0u, d == 3 ? h : 0u}; }
  STAMP_DECL
  STAMP(0);
  Ctx c; c.xp = xp; c.wop = wop; c.w1p = w1p; c.w2p = w2p; c.outp = outp; c.wsp = wsp; c.lds = lds; c.ws = ws; c.b = b; c.l = l; c.w = w;
  c.xg = xg; c.idx = idx; c.kvh = kvh; c.sp = sp; c.r8 = r8; c.c8 = c8; c.jm = jm; c.fidx = fidx; c.m = m;

  if (w < 8) {
    const rsrc_t rk = mk((const char*)kp + (size_t)(kvh * 16 + sp) * 131072, 131072);
    const rsrc_t rv = mk((const char*)vp + (size_t)(kvh * 16 + sp) * 131072, 131072);
    const int ko = w * 4096 + (l & 3) * 1024 + ((l >> 2) & 1) * 256 + ((l >> 3) & 1) * 64 + (l >> 4) * 16;
    const int vo = w * 4096 + l * 16;
    u32x4 Kr[16], Vr[16];
#define KV_LOAD(i) do { if ((i) < 32) { const int j_ = (i) & 15; pin(); \
      if ((i) < 16) Kr[j_] = ld128<KV_AUX>(rk, ko + ((j_ >> 1) & 1) * 512 + (j_ & 1) * 128, (j_ >> 2) * 32768); \
      else Vr[j_] = ld128<KV_AUX>(rv, vo + (j_ & 3) * 1024, (j_ >> 2) * 32768); pin(); } } while (0)
    KV_LOAD(0);
    bar();
#pragma unroll
    for (int i = 1; i < NTHR + NEXTRA; ++i) {
      KV_LOAD(i);
      if (i < 15 && i >= WIN0 - 1) { asm volatile("s_waitcnt vmcnt(%0)" :: "n"(WIN0 - 1) : "memory"); pin(); }
      if (i >= 15 && i < NTHR - 1) { asm volatile("s_waitcnt vmcnt(%0)" :: "n"(WIN - 1) : "memory"); pin(); }
    }
    STAMP(1);
    bar();
    STAMP(2);
    const int qo = L_Q + ((l & 15) >> 3) * 256 + (l & 1) * 64 + (l >> 4) * 16;
    constexpr int N0 = NTHR + NEXTRA, REM = 32 - N0, PER = (REM + 2) / 3;
    f32x4 dx = {0.f, 0.f, 0.f, 0.f}, dy = {0.f, 0.f, 0.f, 0.f};
#pragma unroll
    for (int G = 0; G < 4; ++G) {
      const int qa = ((l >> 1) & 3) == G ? qo : L_ZERO;
      const u32x4 q0 = *(const u32x4*)(lds + qa), q1 = *(const u32x4*)(lds + qa + 128);
      dx = mfma(Kr[4 * G], q0, dx); dy = mfma(Kr[4 * G + 2], q0, dy);
      dx = mfma(Kr[4 * G + 1], q1, dx); dy = mfma(Kr[4 * G + 3], q1, dy);
    }
#pragma unroll
    for (int k = 0; k < PER; ++k) KV_LOAD(N0 + k);
    float S[4];
#pragma unroll
    for (int r = 0; r < 4; ++r) {
      auto s = __builtin_amdgcn_permlane32_swap(asu(dx[r]), asu(dy[r]), false, false);
      const float raw = dppf<0xA0>(asf(s[0])) + dppf<0xF5>(asf(s[1]));
      S[r] = rbf(rbf(raw) * 0.08838834764831845f);
    }
    float mx = fmaxf(fmaxf(S[0], S[1]), fmaxf(S[2], S[3]));
    mx = fmaxf(mx, dppf<XOR2>(mx)); mx = fmaxf(mx, dppf<HMIR>(mx)); mx = x16_max(mx);
#pragma unroll
    for (int k = 0; k < PER; ++k) KV_LOAD(N0 + PER + k);
    float ls = 0.f; u32 Plo[4], Phi[4];
#pragma unroll
    for (int i = 0; i < 4; ++i) {
      const float p = fexp(S[i] - mx); ls += p; const u32 d = pk2(p, p);
      Plo[i] = (u32)__builtin_amdgcn_update_dpp((int)d, (int)d, 0x114, 0xf, 0xa, false);
      Phi[i] = (u32)__builtin_amdgcn_update_dpp((int)d, (int)d, 0x104, 0xf, 0x5, false);
    }
    ls += dppf<XOR2>(ls); ls += dppf<HMIR>(ls); ls = x16_sum(ls);
    if (l == 0 || l == 8) { ((float*)(lds + L_MAX))[(l >> 3) * 8 + w] = mx; ((float*)(lds + L_SUM))[(l >> 3) * 8 + w] = ls; }
#pragma unroll
    for (int k = 0; k < PER; ++k) KV_LOAD(N0 + 2 * PER + k);
    bar();
    STAMP(3);
    u32x4 T1[8], T2[8];
    {
      const rsrc_t r1 = mk(role == 0 ? wop : w1p, role == 0 ? NQ * D * 2 : role == 1 ? D * FF * 2 : 0);
      const int st1 = role == 0 ? D * 2 : FF * 2;
      const int o1 = (role == 0 ? 512 * xg * D * 2 + 128 * idx : 128 * jm) + (64 * w + 8 * r8) * st1 + 16 * c8;
      f32x4 oacc = {0.f, 0.f, 0.f, 0.f};
#pragma unroll
      for (int i = 0; i < 16; ++i) {
        const u32 src = i < 8 ? Plo[i & 3] : Phi[i & 3];
        oacc = mfma(diag((i & 4) ? dppu<0xAA>(src) : dppu<0x00>(src), m), Vr[i], oacc);
        if (i >= 15 - NTEARLY && i < 15) { pin(); T1[i - (15 - NTEARLY)] = ld128<W_AUX>(r1, o1, (i - (15 - NTEARLY)) * st1); pin(); }
      }
      *(f32x4*)(lds + L_ORED + w * 1024 + l * 16) = oacc;
    }
    STAMP(4);
    bar();
    if (role == 0) {
      bar();
      const int head = 4 * xg + (w >> 1);
      const u32x2 ml = ld64<SC1>(ws, WS_ML + ((head >> 1) * 16 + (l & 15)) * 128 + (head & 1) * 8, 0);
      pin();
#pragma unroll
      for (int k = NTEARLY; k < 8; ++k) tail_load<0>(c, k, T1, T2);
      pin();
      tail_wo(c, ml, T1);
    } else if (role == 1) {
      bar();
#pragma unroll
      for (int k = NTEARLY; k < 16; ++k) tail_load<1>(c, k, T1, T2);
      pin();
      tail_mlp(c, T1, T2);
    }
  } else if (w == 8) {
    __builtin_amdgcn_s_setprio(3);
    const u32 ep = ld32<SC1>(ws, WS_EPOCH, 0);
    if (b < 216) {
      const int cb = b >> 2, ks = b & 3;
      const rsrc_t rc = mk(cp, D * 2), rm = mk(wmp, D * NMOD * 2);
      u32x4 Wt[18];
      const u32 c0 = ld32<0>(rc, 288 * ks + l * 4, 0), c1 = ld32<0>(rc, 288 * ks + 256 + r8 * 4, 0);
      const int o = ((144 * ks + 16 * r8) * NMOD + 64 * cb + 8 * c8) * 2, o2 = ((144 * ks + 128 + 2 * r8) * NMOD + 64 * cb + 8 * c8) * 2;
#pragma unroll
      for (int j = 0; j < 16; ++j) Wt[j] = ld128<W_AUX>(rm, o, (2 * (j & 7) + (j >> 3)) * NMOD * 2);
      Wt[16] = ld128<W_AUX>(rm, o2, 0); Wt[17] = ld128<W_AUX>(rm, o2, NMOD * 2);
      if (b < 72 && l < 16) st128(ws, WS_WP + b * 256 + l * 16, u32x4{SENT, SENT, SENT, SENT});
      pin();
      bar();
      STAMP(3);
      const float s0 = silu(lo16(c0)), s1 = silu(hi16(c0)), s2 = silu(lo16(c1)), s3 = silu(hi16(c1));
      f32x4 a = gemv8(pk2(s0, s0), Wt, m);
      a = gemv8(pk2(s1, s1), Wt + 8, m, a);
      a = mfma(diag(pk2(s2, s2), m), Wt[16], a);
      a = mfma(diag(pk2(s3, s3), m), Wt[17], a);
      a = fold(a);
      const u32 tg = __builtin_amdgcn_readfirstlane(ep) + 1u;
      pin();
      asm volatile("s_waitcnt vmcnt(0)" ::: "memory");
      pub_gran(ws, WS_MODP + (ks * NMOD + 64 * cb) * 8, a, tg, (tg * 0x9E3779B1u) >> 24, l);
    } else bar();
    const u32 tag = __builtin_amdgcn_readfirstlane(ep) + 1u;
    const u32 t8 = (tag * 0x9E3779B1u) >> 24;
    STAMP(1);
    if (role == 3) {
      const int mf = b - 168;
      if (mf >= 0 && mf < 36) {
        u32x4 v[2];
        poll_gran<2>([&](int i) { return ld128<SC1>(ws, WS_MODP + ((l >> 5) * 2 * NMOD + 1152 + 64 * mf + 2 * (l & 31)) * 8, i * NMOD * 8); }, v, tag, t8);
        const float s0 = asf(v[0].x & ~255u) + asf(v[1].x & ~255u), s1 = asf(v[0].z & ~255u) + asf(v[1].z & ~255u);
        auto t0 = __builtin_amdgcn_permlane32_swap(asu(s0), asu(s0), false, false);
        auto t1 = __builtin_amdgcn_permlane32_swap(asu(s1), asu(s1), false, false);
        if (l < 32) __builtin_amdgcn_raw_buffer_store_b64(u32x2{pk2(asf(t0[0]) + asf(t0[1]), asf(t1[0]) + asf(t1[1])), tag}, ws, WS_MODF + (32 * mf + l) * 8, 0, SC1);
      }
      STAMP(2);
      bar();
    } else if (role == 0) {
      bar();
    } else if (role == 1) {
      u32x4 v[7];
      _Pragma("unroll 1") for (int spin = 0; spin < SPIN_CAP; ++spin) {
        __builtin_amdgcn_s_sleep(16);
        asm volatile("" ::: "memory");
        bool ok = true;
#pragma unroll
        for (int i = 0; i < 7; ++i) v[i] = ld128<SC1>(ws, WS_MODF + l * 16, i * 1024);
#pragma unroll
        for (int i = 0; i < 7; ++i) ok &= v[i].y == tag && v[i].w == tag;
        if (__all(ok)) break;
      }
#pragma unroll
      for (int i = 0; i < 7; ++i) *(u32x2*)(lds + L_MODV + i * 512 + l * 8) = u32x2{v[i].x, v[i].z};
      STAMP(2);
      bar();
      bar();
      __builtin_amdgcn_s_setprio(0);
      u32x4 T1[8], T2[8];
      bar();
      bar();
#pragma unroll
      for (int k = 0; k < 16; ++k) tail_load<1>(c, k, T1, T2);
      pin();
      tail_mlp(c, T1, T2);
    } else {
      const int col = 32 * fidx + 4 * c8;
      u32x4 gav, gbv;
      _Pragma("unroll 1") for (int spin = 0; spin < SPIN_CAP; ++spin) {
        __builtin_amdgcn_s_sleep(16);
        asm volatile("" ::: "memory");
        gav = ld128<SC1>(ws, WS_MODF + col * 4, 0); gbv = ld128<SC1>(ws, WS_MODF + col * 4, 1728 * 4);
        if (__all(gav.y == tag && gav.w == tag && gbv.y == tag && gbv.w == tag)) break;
      }
      const u32x2 xi = *(const u32x2*)((const char*)xp + col * 2);
      STAMP(2);
      bar();
      bar();
      bar();
      u32x4 wv;
      _Pragma("unroll 1") for (int spin = 0; spin < SPIN_CAP; ++spin) {
        asm volatile("" ::: "memory");
        wv = ld128<SC1>(ws, WS_WP + (r8 * D + col) * 4, 0);
        if (__all(max(max(wv.x, wv.y), max(wv.z, wv.w)) != SENT)) break;
      }
      STAMP(5);
      float x1[4]; const float ga[4] = {lo16(gav.x), hi16(gav.x), lo16(gav.z), hi16(gav.z)}, gb[4] = {lo16(gbv.x), hi16(gbv.x), lo16(gbv.z), hi16(gbv.z)};
#pragma unroll
      for (int i = 0; i < 4; ++i) {
        float t = asf(wv[i]); t += dppf<ROR8>(t); t = x16_sum(t);
        const float xin = (i & 1) ? hi16(xi[i >> 1]) : lo16(xi[i >> 1]);
        x1[i] = rbf(xin + rbf(ga[i] * rbf(t)));
      }
      u32x4 v[9];
      _Pragma("unroll 1") for (int spin = 0; spin < SPIN_CAP; ++spin) {
        asm volatile("" ::: "memory");
#pragma unroll
        for (int s = 0; s < 9; ++s) v[s] = ld128<SC1>(ws, WS_YP + (r8 * D + col) * 4, s * 8 * D * 4);
        u32 mxv = 0;
#pragma unroll
        for (int s = 0; s < 9; ++s) mxv = max(max(mxv, max(v[s].x, v[s].y)), max(v[s].z, v[s].w));
        if (__all(mxv != SENT)) break;
      }
      STAMP(6);
      float r[4];
#pragma unroll
      for (int i = 0; i < 4; ++i) {
        float t = (((asf(v[0][i]) + asf(v[1][i])) + (asf(v[2][i]) + asf(v[3][i]))) + ((asf(v[4][i]) + asf(v[5][i])) + (asf(v[6][i]) + asf(v[7][i])))) + asf(v[8][i]);
        t += dppf<ROR8>(t); t = x16_sum(t);
        r[i] = x1[i] + rbf(gb[i] * rbf(t));
      }
      if (l < 8) *(u32x2*)((char*)outp + col * 2) = u32x2{pk2(r[0], r[1]), pk2(r[2], r[3])};
      if (fidx == 0 && l == 0) __builtin_amdgcn_raw_buffer_store_b32(tag, ws, WS_EPOCH, 0, SC1);
      STAMP(7);
    }
  } else {
    __builtin_amdgcn_s_setprio(2);
    const u32 ep = ld32<SC1>(ws, WS_EPOCH, 0);
    const int cb = 8 * xg + (idx >> 2), ks = idx & 3;
    const rsrc_t rx = mk(xp, D * 2), rq = mk(wqp, D * NQ * 2);
    u32 xa[5]; u32x4 Wt[18];
#pragma unroll
    for (int i = 0; i < 5; ++i) xa[i] = ld32<0>(rx, l * 4 + i * 256, 0);
    const u32 x0 = ld32<0>(rx, 288 * ks + l * 4, 0), x1 = ld32<0>(rx, 288 * ks + 256 + r8 * 4, 0);
    *(u32*)(lds + L_ZERO + l * 4) = 0u;
    pin();
    bar();
    const int o = ((144 * ks + 16 * r8) * NQ + 64 * cb + 8 * c8) * 2, o2 = ((144 * ks + 128 + 2 * r8) * NQ + 64 * cb + 8 * c8) * 2;
#pragma unroll
    for (int j = 0; j < 16; ++j) Wt[j] = ld128<W_AUX>(rq, o, (2 * (j & 7) + (j >> 3)) * NQ * 2);
    Wt[16] = ld128<W_AUX>(rq, o2, 0); Wt[17] = ld128<W_AUX>(rq, o2, NQ * 2);
    { const int ch = cb >> 2, cs = idx & 15; const u32x4 se = {SENT, SENT, SENT, SENT};
      st128(ws, WS_OP + ((2 * ch + (l >> 5)) * 16 + cs) * 512 + (l & 31) * 16, se);
      if (l < 42) st128(ws, l == 41 ? WS_ML + (ch * 16 + cs) * 128 : WS_YP + b * YCLR + l * 16, se); }
    pin();
    float sum = 0.f;
#pragma unroll
    for (int i = 0; i < 5; ++i) sum += lo16(xa[i]) + hi16(xa[i]);
    const float mean = wave_sum(sum) * (1.f / D);
    float sq = 0.f;
#pragma unroll
    for (int i = 0; i < 5; ++i) { const float d0 = lo16(xa[i]) - mean, d1 = hi16(xa[i]) - mean; sq += (i < 4 || l < 32) ? d0 * d0 + d1 * d1 : 0.f; }
    const float var = rbf(wave_sum(sq) * (1.f / D)), mb = rbf(mean);
    const float rsd = 1.f / rbf(__builtin_sqrtf(rbf(var + 1e-5f)));
    float ln[4] = {rbf(rbf(lo16(x0) - mb) * rsd), rbf(rbf(hi16(x0) - mb) * rsd), rbf(rbf(lo16(x1) - mb) * rsd), rbf(rbf(hi16(x1) - mb) * rsd)};
    const u32 tag = __builtin_amdgcn_readfirstlane(ep) + 1u;
    const u32 t8 = (tag * 0x9E3779B1u) >> 24;
    asm volatile("" : "+v"(ln[0]), "+v"(ln[1]), "+v"(ln[2]), "+v"(ln[3]));
    pin();
    asm volatile("s_waitcnt vmcnt(0)" ::: "memory");
    STAMP(1);
    {
      u32x4 v[9];
      const int g0 = WS_MODP + 144 * ks * 8 + l * 16, g1 = WS_MODP + ((c8 & 3) * NMOD + (c8 >> 2) * D + 144 * ks + 128 + 2 * r8) * 8;
      poll_gran<9>([&](int i) { return i < 8 ? ld128<SC1>(ws, g0, ((i & 3) * NMOD + (i >> 2) * D) * 8) : ld128<SC1>(ws, g1, 0); }, v, tag, t8);
      STAMP(2);
      float h[4];
#pragma unroll
      for (int e = 0; e < 2; ++e) {
        const float sa = rbf((gv(v[0][2 * e]) + gv(v[1][2 * e])) + (gv(v[2][2 * e]) + gv(v[3][2 * e])));
        const float ca = rbf((gv(v[4][2 * e]) + gv(v[5][2 * e])) + (gv(v[6][2 * e]) + gv(v[7][2 * e])));
        h[e] = rbf(ln[e] * rbf(1.f + ca)) + sa;
        float t = gv(v[8][2 * e]); t += dppf<XOR1>(t); t += dppf<XOR2>(t); t = rbf(t);
        const float u = dppf<HMIR>(t);
        h[2 + e] = rbf(ln[2 + e] * rbf(1.f + (c8 < 4 ? u : t))) + (c8 < 4 ? t : u);
      }
      f32x4 a = gemv8(pk2(h[0], h[0]), Wt, m);
      a = gemv8(pk2(h[1], h[1]), Wt + 8, m, a);
      a = mfma(diag(pk2(h[2], h[2]), m), Wt[16], a);
      a = mfma(diag(pk2(h[3], h[3]), m), Wt[17], a);
      a = fold(a);
      pub_gran(ws, WS_QP + (ks * NQ + 64 * cb) * 8, a, tag, t8, l);
    }
    STAMP(3);
    {
      u32x4 v[9];
      const int g0 = WS_QP + 256 * kvh * 8 + l * 16, g1 = WS_QP + ((l & 3) * NQ + 256 * (kvh ^ 1) + 64 * ((l >> 2) & 3)) * 8;
      poll_gran<9>([&](int i) { return i < 8 ? ld128<SC1>(ws, g0, (i & 3) * NQ * 8 + (i >> 2) * 1024) : ld128<SC1>(ws, g1, 0); }, v, tag, t8);
#pragma unroll
      for (int i = 0; i < 2; ++i) {
        const float s0 = (gv(v[4 * i].x) + gv(v[4 * i + 1].x)) + (gv(v[4 * i + 2].x) + gv(v[4 * i + 3].x));
        const float s1 = (gv(v[4 * i].z) + gv(v[4 * i + 1].z)) + (gv(v[4 * i + 2].z) + gv(v[4 * i + 3].z));
        *(u32*)(lds + L_Q + i * 256 + l * 4) = pk2(s0, s1);
      }
    }
    STAMP(4);
    bar();
    bar();
    float f[8];
    { const int ho = (l >> 5) * 32;
      const f32x4 m0 = *(const f32x4*)(lds + L_MAX + ho), m1 = *(const f32x4*)(lds + L_MAX + ho + 16);
      const f32x4 l0 = *(const f32x4*)(lds + L_SUM + ho), l1 = *(const f32x4*)(lds + L_SUM + ho + 16);
      const float M = fmaxf(fmaxf(fmaxf(m0[0], m0[1]), fmaxf(m0[2], m0[3])), fmaxf(fmaxf(m1[0], m1[1]), fmaxf(m1[2], m1[3])));
      float L = 0.f;
#pragma unroll
      for (int i = 0; i < 4; ++i) { f[i] = fexp(m0[i] - M); f[4 + i] = fexp(m1[i] - M); L += f[i] * l0[i] + f[4 + i] * l1[i]; }
      if ((l & 31) == 0) __builtin_amdgcn_raw_buffer_store_b64(u32x2{nosent(M), nosent(L)}, ws, WS_ML + (kvh * 16 + sp) * 128 + (l >> 5) * 8, 0, SC1); }
    pin();
    bar();
    f32x4 t[8];
#pragma unroll
    for (int i = 0; i < 8; ++i) t[i] = *(const f32x4*)(lds + L_ORED + i * 1024 + l * 16);
    const f32x4 a = ((t[0] * f[0] + t[1] * f[1]) + (t[2] * f[2] + t[3] * f[3])) + ((t[4] * f[4] + t[5] * f[5]) + (t[6] * f[6] + t[7] * f[7]));
    st128(ws, WS_OP + ((2 * kvh + (l >> 5)) * 16 + sp) * 512 + (l & 15) * 32 + ((l >> 4) & 1) * 16, nosent4(a));
    pin();
    STAMP(5);
    if (role < 2) bar();
    if (role == 0) {
      bar();
      f32x4 r[4];
#pragma unroll
      for (int i = 0; i < 4; ++i) r[i] = *(const f32x4*)(lds + L_RED + ((l >> 4) * 4 + i) * 256 + (l & 15) * 16);
      f32x4 s = (r[0] + r[1]) + (r[2] + r[3]);
#pragma unroll
      for (int i = 0; i < 4; ++i) s[i] = x16_sum(s[i]);
      if (l < 16) st128(ws, WS_WP + (xg * D + 64 * idx) * 4 + l * 16, nosent4(s));
      STAMP(6);
    }
  }
  STAMP_DUMP();
}

extern "C" void kernel_launch(void* const* d_in, const int* in_sizes, int n_in, void* d_out, int out_size, void* d_ws, size_t ws_size,
                              hipStream_t stream) {
  fused_k<<<256, 640, 0, stream>>>(d_in[0], d_in[1], d_in[2], d_in[3], d_in[4], d_in[6], d_in[7], d_in[8], d_in[9], d_out, d_ws);
}
```

```cpp
#define PROBE_K 0
#define PROBE_M 0
#define PROBE_W(w, role) (w == 9 && role != 1)
#include <hip/hip_runtime.h>
#include <stdint.h>

typedef unsigned u32;
typedef unsigned long long u64;
typedef unsigned u32x4 __attribute__((ext_vector_type(4)));
typedef unsigned u32x2 __attribute__((ext_vector_type(2)));
typedef float f32x4 __attribute__((ext_vector_type(4)));
typedef __bf16 bf16x8 __attribute__((ext_vector_type(8)));
typedef __bf16 bf16x2 __attribute__((ext_vector_type(2)));
typedef __amdgpu_buffer_rsrc_t rsrc_t;

#define KV_AUX 2
#define W_AUX 2
#define WIN 3
#define NEXTRA 2
#define NTEARLY 3
#define WIN0 3
#define POLL2 0
#define NTHR 24

constexpr int D = 576, SK = 8192, FF = 4608, NMOD = 3456, NQ = 4096;
constexpr int SC1 = 16;
constexpr u32 SENT = 0xFFFFFFFFu;
constexpr int SPIN_CAP = 1 << 17;

constexpr int WS_EPOCH = 0;
constexpr int WS_MODP = 4096;
constexpr int WS_MODF = 4096 + 110592;
constexpr int WS_QP = 131072;
constexpr int WS_CLR = 262144;
constexpr int WS_OP = WS_CLR;
constexpr int WS_ML = WS_OP + 262144;
constexpr int WS_WP = WS_ML + 32768;
constexpr int WS_YP = WS_WP + 18432;
constexpr int WS_CLR_END = WS_YP + 72 * 2304;
constexpr int YCLR = 656;
static_assert(256 * YCLR >= 72 * 2304, "clear covers the region");
constexpr int WS_STAMP = 1 << 20;
constexpr int WS_BYTES = 2 << 20;
constexpr int OOB = 0x7FFFFFF0;

constexpr int L_Q = 0;
constexpr int L_MAX = 512;
constexpr int L_SUM = 576;
constexpr int L_STAT = 640;
constexpr int L_ORED = 768;
constexpr int L_RED = L_ORED + 8192;
constexpr int L_MODV = L_RED + 4608;
constexpr int L_WGT = L_MODV + 3584;
constexpr int L_ZERO = L_WGT + 256;
constexpr int L_BYTES = L_ZERO + 256;

__device__ __forceinline__ rsrc_t mk(const void* p, u32 bytes) { return __builtin_amdgcn_make_buffer_rsrc((void*)p, 0, bytes, 0x00020000); }
template <int AUX> __device__ __forceinline__ u32x4 ld128(rsrc_t r, int v, int s) { return __builtin_amdgcn_raw_buffer_load_b128(r, v, s, AUX); }
template <int AUX> __device__ __forceinline__ u32x2 ld64(rsrc_t r, int v, int s) { return __builtin_amdgcn_raw_buffer_load_b64(r, v, s, AUX); }
template <int AUX> __device__ __forceinline__ u32 ld32(rsrc_t r, int v, int s) { return __builtin_amdgcn_raw_buffer_load_b32(r, v, s, AUX); }
__device__ __forceinline__ void st128(rsrc_t r, int v, u32x4 x) { __builtin_amdgcn_raw_buffer_store_b128(x, r, v, 0, SC1); }
__device__ __forceinline__ float asf(u32 x) { return __builtin_bit_cast(float, x); }
__device__ __forceinline__ u32 asu(float x) { return __builtin_bit_cast(u32, x); }
__device__ __forceinline__ u32 pk2(float a, float b) { bf16x2 v = {(__bf16)a, (__bf16)b}; return __builtin_bit_cast(u32, v); }
__device__ __forceinline__ float rbf(float a) { return (float)(__bf16)a; }
__device__ __forceinline__ float lo16(u32 x) { return asf(x << 16); }
__device__ __forceinline__ float hi16(u32 x) { return asf(x & 0xFFFF0000u); }
template <int C> __device__ __forceinline__ u32 dppu(u32 x) { return (u32)__builtin_amdgcn_mov_dpp((int)x, C, 0xf, 0xf, true); }
template <int C> __device__ __forceinline__ float dppf(float x) { return asf(dppu<C>(asu(x))); }
constexpr int XOR1 = 0xB1, XOR2 = 0x4E, HMIR = 0x141, ROR8 = 0x128, ROR4 = 0x124;
__device__ __forceinline__ float fexp(float x) { return __builtin_amdgcn_exp2f(x * 1.4426950408889634f); }
__device__ __forceinline__ float silu(float x) { return x * __builtin_amdgcn_rcpf(1.f + fexp(-x)); }
__device__ __forceinline__ f32x4 mfma(u32x4 a, u32x4 b, f32x4 c) {
  return __builtin_amdgcn_mfma_f32_16x16x32_bf16(__builtin_bit_cast(bf16x8, a), __builtin_bit_cast(bf16x8, b), c, 0, 0, 0);
}
__device__ __forceinline__ float x16_sum(float x) {
  auto s = __builtin_amdgcn_permlane16_swap(asu(x), asu(x), false, false);
  x = asf(s[0]) + asf(s[1]);
  auto t = __builtin_amdgcn_permlane32_swap(asu(x), asu(x), false, false);
  return asf(t[0]) + asf(t[1]);
}
__device__ __forceinline__ float x16_max(float x) {
  auto s = __builtin_amdgcn_permlane16_swap(asu(x), asu(x), false, false);
  x = fmaxf(asf(s[0]), asf(s[1]));
  auto t = __builtin_amdgcn_permlane32_swap(asu(x), asu(x), false, false);
  return fmaxf(asf(t[0]), asf(t[1]));
}
__device__ __forceinline__ float row_sum(float x) {
  x += dppf<XOR1>(x); x += dppf<XOR2>(x); x += dppf<HMIR>(x); x += dppf<ROR8>(x); return x;
}
__device__ __forceinline__ float row_max(float x) {
  x = fmaxf(x, dppf<XOR1>(x)); x = fmaxf(x, dppf<XOR2>(x)); x = fmaxf(x, dppf<HMIR>(x)); x = fmaxf(x, dppf<ROR8>(x)); return x;
}
__device__ __forceinline__ float wave_sum(float x) { return x16_sum(row_sum(x)); }
__device__ __forceinline__ void bar() { asm volatile("s_waitcnt lgkmcnt(0)\n\ts_barrier" ::: "memory"); }
__device__ __forceinline__ void pin() { __builtin_amdgcn_sched_barrier(0); }
__device__ __forceinline__ u32x4 diag(u32 dup, const u32x4& m) { return u32x4{dup & m.x, dup & m.y, dup & m.z, dup & m.w}; }
__device__ __forceinline__ f32x4 fold(f32x4 a) {
  f32x4 r;
#pragma unroll
  for (int i = 0; i < 4; ++i) {
    const u32 t = dppu<ROR8>(asu(a[i]));
    auto s = __builtin_amdgcn_permlane32_swap(t, t, false, false);
    r[i] = a[i] + asf(s[1]);
  }
  return r;
}
__device__ __forceinline__ f32x4 gemv8(u32 dup, const u32x4* w, const u32x4& m, f32x4 a = f32x4{0.f, 0.f, 0.f, 0.f}) {
  const u32 lo = (u32)__builtin_amdgcn_update_dpp((int)dup, (int)dup, 0x114, 0xf, 0xa, false);
  const u32 hi = (u32)__builtin_amdgcn_update_dpp((int)dup, (int)dup, 0x104, 0xf, 0x5, false);
  a = mfma(diag(dppu<0x00>(lo), m), w[0], a);
  a = mfma(diag(dppu<0x55>(lo), m), w[1], a);
  a = mfma(diag(dppu<0xAA>(lo), m), w[2], a);
  a = mfma(diag(dppu<0xFF>(lo), m), w[3], a);
  a = mfma(diag(dppu<0x00>(hi), m), w[4], a);
  a = mfma(diag(dppu<0x55>(hi), m), w[5], a);
  a = mfma(diag(dppu<0xAA>(hi), m), w[6], a);
  a = mfma(diag(dppu<0xFF>(hi), m), w[7], a);
  return a;
}
__device__ __forceinline__ u32 nosent(float x) { return min(asu(x), 0xFFFFFFFEu); }
__device__ __forceinline__ u32x4 nosent4(f32x4 a) { return u32x4{nosent(a[0]), nosent(a[1]), nosent(a[2]), nosent(a[3])}; }
__device__ __forceinline__ bool gran_ok(u32x4 x, u32 tag, u32 t8) { return x.y == tag && x.w == tag && (x.x & 255u) == t8 && (x.z & 255u) == t8; }
__device__ __forceinline__ void pub_gran(rsrc_t ws, int base, f32x4 a, u32 tag, u32 t8, int l) {
  if (l < 32 && (l & 15) < 8) {
    const int o = base + (8 * (l & 7) + 4 * (l >> 4)) * 8;
    st128(ws, o, u32x4{(asu(a[0]) & ~255u) | t8, tag, (asu(a[1]) & ~255u) | t8, tag});
    st128(ws, o + 16, u32x4{(asu(a[2]) & ~255u) | t8, tag, (asu(a[3]) & ~255u) | t8, tag});
  }
}
template <int N> __device__ __forceinline__ bool gran_all(const u32x4 (&v)[N], u32 tag, u32 t8) {
  u32 a = 0, bq = 0;
#pragma unroll
  for (int i = 0; i < N; ++i) { a |= (v[i].y ^ tag) | (v[i].w ^ tag); bq |= (v[i].x ^ t8) | (v[i].z ^ t8); }
  return __all((a | (bq & 255u)) == 0u);
}
template <int N, class F> __device__ __forceinline__ void poll_gran(F ld, u32x4 (&v)[N], u32 tag, u32 t8) {
  _Pragma("unroll 1") for (int spin = 0; spin < SPIN_CAP; ++spin) {
    asm volatile("" ::: "memory");
#pragma unroll
    for (int i = 0; i < N; ++i) v[i] = ld(i);
    if (gran_all<N>(v, tag, t8)) break;
  }
}
__device__ __forceinline__ float gv(u32 x) { return asf(x & ~255u); }
#define STAMP(k) do { if ((k) == 0 || (k) == PROBE_K) { pin(); const u32 t_ = (u32)__builtin_amdgcn_s_memrealtime(); if (l == 0) stl[w * 2 + ((k) != 0)] = t_; pin(); } } while (0)
#define STAMP_DECL __shared__ u32 stl[32]; if (l < 2) stl[w * 2 + l] = 0;
#define STAMP_DUMP() do { const u32 t0_ = stl[w * 2], t1_ = PROBE_K ? stl[w * 2 + 1] : t0_; if ((PROBE_W(w, role)) && t1_ != 0) { const u32 e_ = 6000u + PROBE_M * (t1_ - t0_); \
    while ((u32)__builtin_amdgcn_s_memrealtime() - t0_ < e_) __builtin_amdgcn_s_sleep(4); } } while (0)

#define END() do { STAMP_DUMP(); __builtin_amdgcn_endpgm(); } while (0)
struct Ctx { const void *xp, *wop, *w1p, *w2p; void *outp, *wsp; unsigned char* lds; rsrc_t ws; int b, l, w, xg, idx, kvh, sp, r8, c8, jm, fidx; u32x4 m;
  u32* stl;
};
#define CTX_STAMP u32* stl = c.stl; const int l = c.l, w = c.w; (void)l; (void)w;

template <int ROLE> __device__ __forceinline__ void tail_load(const Ctx& c, int k, u32x4 (&T1)[8], u32x4 (&T2)[8]) {
  if (ROLE == 0 && k < 8) T1[k] = ld128<W_AUX>(mk(c.wop, NQ * D * 2), ((512 * c.xg + 64 * c.w + 8 * c.r8) * D + 64 * c.idx + 8 * c.c8) * 2, k * D * 2);
  if (ROLE == 1 && k < 8) T1[k] = ld128<W_AUX>(mk(c.w1p, D * FF * 2), ((64 * c.w + 8 * c.r8) * FF + 64 * c.jm + 8 * c.c8) * 2, k * FF * 2);
  if (ROLE == 1 && k >= 8) T2[k - 8] = ld128<W_AUX>(mk(c.w2p, D * FF * 2), ((64 * c.jm + 8 * c.r8) * D + 64 * c.w + 8 * c.c8) * 2, (k - 8) * D * 2);
}

__device__ __forceinline__ void tail_wo(const Ctx& c, u32x2 ml, const u32x4 (&T1)[8]) {
  CTX_STAMP
  const int head = 4 * c.xg + (w >> 1);
  _Pragma("unroll 1") for (int spin = 0; spin < SPIN_CAP; ++spin) {
    if (__all(ml.x != SENT && ml.y != SENT)) break;
    asm volatile("" ::: "memory");
    ml = ld64<SC1>(c.ws, WS_ML + ((head >> 1) * 16 + (l & 15)) * 128 + (head & 1) * 8, 0);
  }
  const float M = row_max(asf(ml.x)), e = fexp(asf(ml.x) - M), L = row_sum(e * asf(ml.y));
  const float wgt = e / L;
  float ws_[16];
#pragma unroll
  for (int s = 0; s < 16; ++s) ws_[s] = asf(__builtin_amdgcn_readlane(asu(wgt), s));
  pin();
  STAMP(8);
  u32 v[16];
  _Pragma("unroll 1") for (int spin = 0; spin < SPIN_CAP; ++spin) {
    asm volatile("" ::: "memory");
#pragma unroll
    for (int s = 0; s < 16; ++s) v[s] = ld32<SC1>(c.ws, WS_OP + head * 8192 + (w & 1) * 256 + l * 4, s * 512);
    u32 mxv = 0;
#pragma unroll
    for (int s = 0; s < 16; ++s) mxv = max(mxv, v[s]);
    if (__all(mxv != SENT)) break;
  }
  STAMP(5);
  float o4[4];
#pragma unroll
  for (int i = 0; i < 4; ++i) o4[i] = fmaf(ws_[i], asf(v[i]), fmaf(ws_[i + 4], asf(v[i + 4]), fmaf(ws_[i + 8], asf(v[i + 8]), ws_[i + 12] * asf(v[i + 12]))));
  const float o = (o4[0] + o4[1]) + (o4[2] + o4[3]);
  const f32x4 a = gemv8(pk2(o, o), T1, c.m);
  if (((l >> 5) ^ (l >> 3)) & 1) {} else *(f32x4*)(c.lds + L_RED + (w * 2 + (l >> 5)) * 256 + (2 * c.c8 + ((l >> 4) & 1)) * 16) = a;
  bar();
  STAMP(6);
}

__device__ __forceinline__ void tail_mlp(const Ctx& c, const u32x4 (&T1)[8], const u32x4 (&T2)[8]) {
  CTX_STAMP
  const int col = 64 * w + l;
  const float xin = asf((u32)((const unsigned short*)c.xp)[col] << 16);
  const unsigned short* mv = (const unsigned short*)(c.lds + L_MODV);
  const float ga = asf((u32)mv[col] << 16), sb = asf((u32)mv[D + col] << 16), cb1 = rbf(1.f + asf((u32)mv[2 * D + col] << 16));
  u32 v[8];
  STAMP(8);
  _Pragma("unroll 1") for (int spin = 0; spin < SPIN_CAP; ++spin) {
    asm volatile("" ::: "memory");
#pragma unroll
    for (int s = 0; s < 8; ++s) v[s] = ld32<SC1>(c.ws, WS_WP + col * 4, s * D * 4);
    u32 mxv = 0;
#pragma unroll
    for (int s = 0; s < 8; ++s) mxv = max(mxv, v[s]);
    if (__all(mxv != SENT)) break;
  }
  STAMP(5);
  const float at = ((asf(v[0]) + asf(v[1])) + (asf(v[2]) + asf(v[3]))) + ((asf(v[4]) + asf(v[5])) + (asf(v[6]) + asf(v[7])));
  const float x1 = rbf(xin + rbf(ga * rbf(at)));
  const float s1 = wave_sum(x1), s2 = wave_sum(x1 * x1);
  if (l == 0) *(float2*)(c.lds + L_STAT + w * 8) = make_float2(s1, s2);
  bar();
  float mean, var;
  { const f32x4 t0 = *(const f32x4*)(c.lds + L_STAT), t1 = *(const f32x4*)(c.lds + L_STAT + 16), t2 = *(const f32x4*)(c.lds + L_STAT + 32),
                t3 = *(const f32x4*)(c.lds + L_STAT + 48); const float2 t4 = *(const float2*)(c.lds + L_STAT + 64);
    const f32x4 t = (t0 + t1) + (t2 + t3);
    mean = ((t[0] + t[2]) + t4.x) * (1.f / D);
    var = fmaxf(((t[1] + t[3]) + t4.y) * (1.f / D) - mean * mean, 0.f); }
  const float rsd = __builtin_amdgcn_rcpf(rbf(__builtin_amdgcn_sqrtf(rbf(rbf(var) + 1e-5f))));
  const float ln = rbf(rbf(x1 - rbf(mean)) * rsd);
  const float h = rbf(ln * cb1) + sb;
  const f32x4 a = gemv8(pk2(h, h), T1, c.m);
  if (((l >> 5) ^ (l >> 3)) & 1) {} else *(f32x4*)(c.lds + L_RED + (w * 2 + (l >> 5)) * 256 + (2 * c.c8 + ((l >> 4) & 1)) * 16) = a;
  STAMP(6);
  bar();
  float u[18];
#pragma unroll
  for (int i = 0; i < 18; ++i) u[i] = *(const float*)(c.lds + L_RED + i * 256 + l * 4);
  const float us = (((u[0] + u[1]) + (u[2] + u[3])) + ((u[4] + u[5]) + (u[6] + u[7]))) + (((u[8] + u[9]) + (u[10] + u[11])) + ((u[12] + u[13]) + (u[14] + u[15]))) + (u[16] + u[17]);
  const float act = silu(rbf(us));
  const f32x4 y = fold(gemv8(pk2(act, act), T2, c.m));
  if (l < 32 && (l & 15) < 8) st128(c.ws, WS_YP + (c.jm * D + 64 * w) * 4 + (2 * c.c8 + (l >> 4)) * 16, nosent4(y));
  STAMP(7);
}

__global__ void __launch_bounds__(640, 3)
fused_k(const void* __restrict__ xp, const void* __restrict__ kp, const void* __restrict__ vp, const void* __restrict__ wqp,
        const void* __restrict__ wop, const void* __restrict__ w1p, const void* __restrict__ w2p, const void* __restrict__ cp,
        const void* __restrict__ wmp, void* __restrict__ outp, void* wsp) {
  __shared__ __attribute__((aligned(16))) unsigned char lds[L_BYTES];
  const int b = blockIdx.x, l = threadIdx.x & 63, w = __builtin_amdgcn_readfirstlane(threadIdx.x >> 6);
  const int xg = b & 7, idx = b >> 3, kvh = xg * 2 + (idx & 1), sp = idx >> 1;
  const int r8 = l >> 3, c8 = l & 7;
  const rsrc_t ws = mk(wsp, WS_BYTES);
  const int fidx = b - 144;
  const int role = idx < 9 ? 0 : idx < 18 ? 1 : fidx < 18 ? 2 : 3;
  const int jm = xg * 9 + idx - 9;
  u32x4 m;
  { const u32 h = (l & 1) ? 0xFFFF0000u : 0x0000FFFFu; const int d = c8 >> 1;
    m = u32x4{d == 0 ? h : 0u, d == 1 ? h : 0u, d == 2 ? h : 0u, d == 3 ? h : 0u}; }
  STAMP_DECL
  STAMP(0);
  Ctx c; c.xp = xp; c.wop = wop; c.w1p = w1p; c.w2p = w2p; c.outp = outp; c.wsp = wsp; c.lds = lds; c.ws = ws; c.b = b; c.l = l; c.w = w;
  c.xg = xg; c.idx = idx; c.kvh = kvh; c.sp = sp; c.r8 = r8; c.c8 = c8; c.jm = jm; c.fidx = fidx; c.m = m;
  c.stl = stl;

  if (w < 8) {
    const rsrc_t rk = mk((const char*)kp + (size_t)(kvh * 16 + sp) * 131072, 131072);
    const rsrc_t rv = mk((const char*)vp + (size_t)(kvh * 16 + sp) * 131072, 131072);
    const int ko = w * 4096 + (l & 3) * 1024 + ((l >> 2) & 1) * 256 + ((l >> 3) & 1) * 64 + (l >> 4) * 16;
    const int vo = w * 4096 + l * 16;
    u32x4 Kr[16], Vr[16];
#define KV_LOAD(i) do { if ((i) < 32) { const int j_ = (i) & 15; pin(); \
      if ((i) < 16) Kr[j_] = ld128<KV_AUX>(rk, ko + ((j_ >> 1) & 1) * 512 + (j_ & 1) * 128, (j_ >> 2) * 32768); \
      else Vr[j_] = ld128<KV_AUX>(rv, vo + (j_ & 3) * 1024, (j_ >> 2) * 32768); pin(); } } while (0)
    KV_LOAD(0);
    bar();
#pragma unroll
    for (int i = 1; i < NTHR + NEXTRA; ++i) {
      KV_LOAD(i);
      if (i < 15 && i >= WIN0 - 1) { asm volatile("s_waitcnt vmcnt(%0)" :: "n"(WIN0 - 1) : "memory"); pin(); }
      if (i >= 15 && i < NTHR - 1) { asm volatile("s_waitcnt vmcnt(%0)" :: "n"(WIN - 1) : "memory"); pin(); }
    }
    STAMP(1);
    bar();
    STAMP(2);
    const int qo = L_Q + ((l & 15) >> 3) * 256 + (l & 1) * 64 + (l >> 4) * 16;
    constexpr int N0 = NTHR + NEXTRA, REM = 32 - N0, PER = (REM + 2) / 3;
    f32x4 dx = {0.f, 0.f, 0.f, 0.f}, dy = {0.f, 0.f, 0.f, 0.f};
#pragma unroll
    for (int G = 0; G < 4; ++G) {
      const int qa = ((l >> 1) & 3) == G ? qo : L_ZERO;
      const u32x4 q0 = *(const u32x4*)(lds + qa), q1 = *(const u32x4*)(lds + qa + 128);
      dx = mfma(Kr[4 * G], q0, dx); dy = mfma(Kr[4 * G + 2], q0, dy);
      dx = mfma(Kr[4 * G + 1], q1, dx); dy = mfma(Kr[4 * G + 3], q1, dy);
    }
#pragma unroll
    for (int k = 0; k < PER; ++k) KV_LOAD(N0 + k);
    float S[4];
#pragma unroll
    for (int r = 0; r < 4; ++r) {
      auto s = __builtin_amdgcn_permlane32_swap(asu(dx[r]), asu(dy[r]), false, false);
      const float raw = dppf<0xA0>(asf(s[0])) + dppf<0xF5>(asf(s[1]));
      S[r] = rbf(rbf(raw) * 0.08838834764831845f);
    }
    float mx = fmaxf(fmaxf(S[0], S[1]), fmaxf(S[2], S[3]));
    mx = fmaxf(mx, dppf<XOR2>(mx)); mx = fmaxf(mx, dppf<HMIR>(mx)); mx = x16_max(mx);
#pragma unroll
    for (int k = 0; k < PER; ++k) KV_LOAD(N0 + PER + k);
    float ls = 0.f; u32 Plo[4], Phi[4];
#pragma unroll
    for (int i = 0; i < 4; ++i) {
      const float p = fexp(S[i] - mx); ls += p; const u32 d = pk2(p, p);
      Plo[i] = (u32)__builtin_amdgcn_update_dpp((int)d, (int)d, 0x114, 0xf, 0xa, false);
      Phi[i] = (u32)__builtin_amdgcn_update_dpp((int)d, (int)d, 0x104, 0xf, 0x5, false);
    }
    ls += dppf<XOR2>(ls); ls += dppf<HMIR>(ls); ls = x16_sum(ls);
    if (l == 0 || l == 8) { ((float*)(lds + L_MAX))[(l >> 3) * 8 + w] = mx; ((float*)(lds + L_SUM))[(l >> 3) * 8 + w] = ls; }
#pragma unroll
    for (int k = 0; k < PER; ++k) KV_LOAD(N0 + 2 * PER + k);
    bar();
    STAMP(3);
    u32x4 T1[8], T2[8];
    {
      const rsrc_t r1 = mk(role == 0 ? wop : w1p, role == 0 ? NQ * D * 2 : role == 1 ? D * FF * 2 : 0);
      const int st1 = role == 0 ? D * 2 : FF * 2;
      const int o1 = (role == 0 ? 512 * xg * D * 2 + 128 * idx : 128 * jm) + (64 * w + 8 * r8) * st1 + 16 * c8;
      f32x4 oacc = {0.f, 0.f, 0.f, 0.f};
#pragma unroll
      for (int i = 0; i < 16; ++i) {
        const u32 src = i < 8 ? Plo[i & 3] : Phi[i & 3];
        oacc = mfma(diag((i & 4) ? dppu<0xAA>(src) : dppu<0x00>(src), m), Vr[i], oacc);
        if (i >= 15 - NTEARLY && i < 15) { pin(); T1[i - (15 - NTEARLY)] = ld128<W_AUX>(r1, o1, (i - (15 - NTEARLY)) * st1); pin(); }
      }
      *(f32x4*)(lds + L_ORED + w * 1024 + l * 16) = oacc;
    }
    STAMP(4);
    bar();
    if (role == 0) {
      bar();
      const int head = 4 * xg + (w >> 1);
      const u32x2 ml = ld64<SC1>(ws, WS_ML + ((head >> 1) * 16 + (l & 15)) * 128 + (head & 1) * 8, 0);
      pin();
#pragma unroll
      for (int k = NTEARLY; k < 8; ++k) tail_load<0>(c, k, T1, T2);
      pin();
      tail_wo(c, ml, T1);
    } else if (role == 1) {
      bar();
#pragma unroll
      for (int k = NTEARLY; k < 16; ++k) tail_load<1>(c, k, T1, T2);
      pin();
      tail_mlp(c, T1, T2);
    }
  } else if (w == 8) {
    __builtin_amdgcn_s_setprio(3);
    const u32 ep = ld32<SC1>(ws, WS_EPOCH, 0);
    if (b < 216) {
      const int cb = b >> 2, ks = b & 3;
      const rsrc_t rc = mk(cp, D * 2), rm = mk(wmp, D * NMOD * 2);
      u32x4 Wt[18];
      const u32 c0 = ld32<0>(rc, 288 * ks + l * 4, 0), c1 = ld32<0>(rc, 288 * ks + 256 + r8 * 4, 0);
      const int o = ((144 * ks + 16 * r8) * NMOD + 64 * cb + 8 * c8) * 2, o2 = ((144 * ks + 128 + 2 * r8) * NMOD + 64 * cb + 8 * c8) * 2;
#pragma unroll
      for (int j = 0; j < 16; ++j) Wt[j] = ld128<W_AUX>(rm, o, (2 * (j & 7) + (j >> 3)) * NMOD * 2);
      Wt[16] = ld128<W_AUX>(rm, o2, 0); Wt[17] = ld128<W_AUX>(rm, o2, NMOD * 2);
      if (b < 72 && l < 16) st128(ws, WS_WP + b * 256 + l * 16, u32x4{SENT, SENT, SENT, SENT});
      pin();
      bar();
      STAMP(3);
      const float s0 = silu(lo16(c0)), s1 = silu(hi16(c0)), s2 = silu(lo16(c1)), s3 = silu(hi16(c1));
      f32x4 a = gemv8(pk2(s0, s0), Wt, m);
      a = gemv8(pk2(s1, s1), Wt + 8, m, a);
      a = mfma(diag(pk2(s2, s2), m), Wt[16], a);
      a = mfma(diag(pk2(s3, s3), m), Wt[17], a);
      a = fold(a);
      const u32 tg = __builtin_amdgcn_readfirstlane(ep) + 1u;
      pin();
      asm volatile("s_waitcnt vmcnt(0)" ::: "memory");
      pub_gran(ws, WS_MODP + (ks * NMOD + 64 * cb) * 8, a, tg, (tg * 0x9E3779B1u) >> 24, l);
    } else bar();
    const u32 tag = __builtin_amdgcn_readfirstlane(ep) + 1u;
    const u32 t8 = (tag * 0x9E3779B1u) >> 24;
    STAMP(1);
    if (role == 3) {
      const int mf = b - 168;
      if (mf >= 0 && mf < 36) {
        u32x4 v[2];
        poll_gran<2>([&](int i) { return ld128<SC1>(ws, WS_MODP + ((l >> 5) * 2 * NMOD + 1152 + 64 * mf + 2 * (l & 31)) * 8, i * NMOD * 8); }, v, tag, t8);
        const float s0 = asf(v[0].x & ~255u) + asf(v[1].x & ~255u), s1 = asf(v[0].z & ~255u) + asf(v[1].z & ~255u);
        auto t0 = __builtin_amdgcn_permlane32_swap(asu(s0), asu(s0), false, false);
        auto t1 = __builtin_amdgcn_permlane32_swap(asu(s1), asu(s1), false, false);
        if (l < 32) __builtin_amdgcn_raw_buffer_store_b64(u32x2{pk2(asf(t0[0]) + asf(t0[1]), asf(t1[0]) + asf(t1[1])), tag}, ws, WS_MODF + (32 * mf + l) * 8, 0, SC1);
      }
      STAMP(2);
      bar();
    } else if (role == 0) {
      bar();
    } else if (role == 1) {
      u32x4 v[7];
      _Pragma("unroll 1") for (int spin = 0; spin < SPIN_CAP; ++spin) {
        __builtin_amdgcn_s_sleep(16);
        asm volatile("" ::: "memory");
        bool ok = true;
#pragma unroll
        for (int i = 0; i < 7; ++i) v[i] = ld128<SC1>(ws, WS_MODF + l * 16, i * 1024);
#pragma unroll
        for (int i = 0; i < 7; ++i) ok &= v[i].y == tag && v[i].w == tag;
        if (__all(ok)) break;
      }
#pragma unroll
      for (int i = 0; i < 7; ++i) *(u32x2*)(lds + L_MODV + i * 512 + l * 8) = u32x2{v[i].x, v[i].z};
      STAMP(2);
      bar();
      bar();
      __builtin_amdgcn_s_setprio(0);
      u32x4 T1[8], T2[8];
      bar();
      bar();
#pragma unroll
      for (int k = 0; k < 16; ++k) tail_load<1>(c, k, T1, T2);
      pin();
      tail_mlp(c, T1, T2);
    } else {
      const int col = 32 * fidx + 4 * c8;
      u32x4 gav, gbv;
      _Pragma("unroll 1") for (int spin = 0; spin < SPIN_CAP; ++spin) {
        __builtin_amdgcn_s_sleep(16);
        asm volatile("" ::: "memory");
        gav = ld128<SC1>(ws, WS_MODF + col * 4, 0); gbv = ld128<SC1>(ws, WS_MODF + col * 4, 1728 * 4);
        if (__all(gav.y == tag && gav.w == tag && gbv.y == tag && gbv.w == tag)) break;
      }
      const u32x2 xi = *(const u32x2*)((const char*)xp + col * 2);
      STAMP(2);
      bar();
      bar();
      bar();
      u32x4 wv;
      _Pragma("unroll 1") for (int spin = 0; spin < SPIN_CAP; ++spin) {
        asm volatile("" ::: "memory");
        wv = ld128<SC1>(ws, WS_WP + (r8 * D + col) * 4, 0);
        if (__all(max(max(wv.x, wv.y), max(wv.z, wv.w)) != SENT)) break;
      }
      STAMP(5);
      float x1[4]; const float ga[4] = {lo16(gav.x), hi16(gav.x), lo16(gav.z), hi16(gav.z)}, gb[4] = {lo16(gbv.x), hi16(gbv.x), lo16(gbv.z), hi16(gbv.z)};
#pragma unroll
      for (int i = 0; i < 4; ++i) {
        float t = asf(wv[i]); t += dppf<ROR8>(t); t = x16_sum(t);
        const float xin = (i & 1) ? hi16(xi[i >> 1]) : lo16(xi[i >> 1]);
        x1[i] = rbf(xin + rbf(ga[i] * rbf(t)));
      }
      u32x4 v[9];
      _Pragma("unroll 1") for (int spin = 0; spin < SPIN_CAP; ++spin) {
        asm volatile("" ::: "memory");
#pragma unroll
        for (int s = 0; s < 9; ++s) v[s] = ld128<SC1>(ws, WS_YP + (r8 * D + col) * 4, s * 8 * D * 4);
        u32 mxv = 0;
#pragma unroll
        for (int s = 0; s < 9; ++s) mxv = max(max(mxv, max(v[s].x, v[s].y)), max(v[s].z, v[s].w));
        if (__all(mxv != SENT)) break;
      }
      STAMP(6);
      float r[4];
#pragma unroll
      for (int i = 0; i < 4; ++i) {
        float t = (((asf(v[0][i]) + asf(v[1][i])) + (asf(v[2][i]) + asf(v[3][i]))) + ((asf(v[4][i]) + asf(v[5][i])) + (asf(v[6][i]) + asf(v[7][i])))) + asf(v[8][i]);
        t += dppf<ROR8>(t); t = x16_sum(t);
        r[i] = x1[i] + rbf(gb[i] * rbf(t));
      }
      if (l < 8) *(u32x2*)((char*)outp + col * 2) = u32x2{pk2(r[0], r[1]), pk2(r[2], r[3])};
      if (fidx == 0 && l == 0) __builtin_amdgcn_raw_buffer_store_b32(tag, ws, WS_EPOCH, 0, SC1);
      STAMP(7);
    }
  } else {
    __builtin_amdgcn_s_setprio(2);
    const u32 ep = ld32<SC1>(ws, WS_EPOCH, 0);
    const int cb = 8 * xg + (idx >> 2), ks = idx & 3;
    const rsrc_t rx = mk(xp, D * 2), rq = mk(wqp, D * NQ * 2);
    u32 xa[5]; u32x4 Wt[18];
#pragma unroll
    for (int i = 0; i < 5; ++i) xa[i] = ld32<0>(rx, l * 4 + i * 256, 0);
    const u32 x0 = ld32<0>(rx, 288 * ks + l * 4, 0), x1 = ld32<0>(rx, 288 * ks + 256 + r8 * 4, 0);
    *(u32*)(lds + L_ZERO + l * 4) = 0u;
    pin();
    bar();
    const int o = ((144 * ks + 16 * r8) * NQ + 64 * cb + 8 * c8) * 2, o2 = ((144 * ks + 128 + 2 * r8) * NQ + 64 * cb + 8 * c8) * 2;
#pragma unroll
    for (int j = 0; j < 16; ++j) Wt[j] = ld128<W_AUX>(rq, o, (2 * (j & 7) + (j >> 3)) * NQ * 2);
    Wt[16] = ld128<W_AUX>(rq, o2, 0); Wt[17] = ld128<W_AUX>(rq, o2, NQ * 2);
    { const int ch = cb >> 2, cs = idx & 15; const u32x4 se = {SENT, SENT, SENT, SENT};
      st128(ws, WS_OP + ((2 * ch + (l >> 5)) * 16 + cs) * 512 + (l & 31) * 16, se);
      if (l < 42) st128(ws, l == 41 ? WS_ML + (ch * 16 + cs) * 128 : WS_YP + b * YCLR + l * 16, se); }
    pin();
    float sum = 0.f;
#pragma unroll
    for (int i = 0; i < 5; ++i) sum += lo16(xa[i]) + hi16(xa[i]);
    const float mean = wave_sum(sum) * (1.f / D);
    float sq = 0.f;
#pragma unroll
    for (int i = 0; i < 5; ++i) { const float d0 = lo16(xa[i]) - mean, d1 = hi16(xa[i]) - mean; sq += (i < 4 || l < 32) ? d0 * d0 + d1 * d1 : 0.f; }
    const float var = rbf(wave_sum(sq) * (1.f / D)), mb = rbf(mean);
    const float rsd = 1.f / rbf(__builtin_sqrtf(rbf(var + 1e-5f)));
    float ln[4] = {rbf(rbf(lo16(x0) - mb) * rsd), rbf(rbf(hi16(x0) - mb) * rsd), rbf(rbf(lo16(x1) - mb) * rsd), rbf(rbf(hi16(x1) - mb) * rsd)};
    const u32 tag = __builtin_amdgcn_readfirstlane(ep) + 1u;
    const u32 t8 = (tag * 0x9E3779B1u) >> 24;
    asm volatile("" : "+v"(ln[0]), "+v"(ln[1]), "+v"(ln[2]), "+v"(ln[3]));
    pin();
    asm volatile("s_waitcnt vmcnt(0)" ::: "memory");
    STAMP(1);
    {
      u32x4 v[9];
      const int g0 = WS_MODP + 144 * ks * 8 + l * 16, g1 = WS_MODP + ((c8 & 3) * NMOD + (c8 >> 2) * D + 144 * ks + 128 + 2 * r8) * 8;
      poll_gran<9>([&](int i) { return i < 8 ? ld128<SC1>(ws, g0, ((i & 3) * NMOD + (i >> 2) * D) * 8) : ld128<SC1>(ws, g1, 0); }, v, tag, t8);
      STAMP(2);
      float h[4];
#pragma unroll
      for (int e = 0; e < 2; ++e) {
        const float sa = rbf((gv(v[0][2 * e]) + gv(v[1][2 * e])) + (gv(v[2][2 * e]) + gv(v[3][2 * e])));
        const float ca = rbf((gv(v[4][2 * e]) + gv(v[5][2 * e])) + (gv(v[6][2 * e]) + gv(v[7][2 * e])));
        h[e] = rbf(ln[e] * rbf(1.f + ca)) + sa;
        float t = gv(v[8][2 * e]); t += dppf<XOR1>(t); t += dppf<XOR2>(t); t = rbf(t);
        const float u = dppf<HMIR>(t);
        h[2 + e] = rbf(ln[2 + e] * rbf(1.f + (c8 < 4 ? u : t))) + (c8 < 4 ? t : u);
      }
      f32x4 a = gemv8(pk2(h[0], h[0]), Wt, m);
      a = gemv8(pk2(h[1], h[1]), Wt + 8, m, a);
      a = mfma(diag(pk2(h[2], h[2]), m), Wt[16], a);
      a = mfma(diag(pk2(h[3], h[3]), m), Wt[17], a);
      a = fold(a);
      pub_gran(ws, WS_QP + (ks * NQ + 64 * cb) * 8, a, tag, t8, l);
    }
    STAMP(3);
    {
      u32x4 v[9];
      const int g0 = WS_QP + 256 * kvh * 8 + l * 16, g1 = WS_QP + ((l & 3) * NQ + 256 * (kvh ^ 1) + 64 * ((l >> 2) & 3)) * 8;
      poll_gran<9>([&](int i) { return i < 8 ? ld128<SC1>(ws, g0, (i & 3) * NQ * 8 + (i >> 2) * 1024) : ld128<SC1>(ws, g1, 0); }, v, tag, t8);
#pragma unroll
      for (int i = 0; i < 2; ++i) {
        const float s0 = (gv(v[4 * i].x) + gv(v[4 * i + 1].x)) + (gv(v[4 * i + 2].x) + gv(v[4 * i + 3].x));
        const float s1 = (gv(v[4 * i].z) + gv(v[4 * i + 1].z)) + (gv(v[4 * i + 2].z) + gv(v[4 * i + 3].z));
        *(u32*)(lds + L_Q + i * 256 + l * 4) = pk2(s0, s1);
      }
    }
    STAMP(4);
    bar();
    bar();
    float f[8];
    { const int ho = (l >> 5) * 32;
      const f32x4 m0 = *(const f32x4*)(lds + L_MAX + ho), m1 = *(const f32x4*)(lds + L_MAX + ho + 16);
      const f32x4 l0 = *(const f32x4*)(lds + L_SUM + ho), l1 = *(const f32x4*)(lds + L_SUM + ho + 16);
      const float M = fmaxf(fmaxf(fmaxf(m0[0], m0[1]), fmaxf(m0[2], m0[3])), fmaxf(fmaxf(m1[0], m1[1]), fmaxf(m1[2], m1[3])));
      float L = 0.f;
#pragma unroll
      for (int i = 0; i < 4; ++i) { f[i] = fexp(m0[i] - M); f[4 + i] = fexp(m1[i] - M); L += f[i] * l0[i] + f[4 + i] * l1[i]; }
      if ((l & 31) == 0) __builtin_amdgcn_raw_buffer_store_b64(u32x2{nosent(M), nosent(L)}, ws, WS_ML + (kvh * 16 + sp) * 128 + (l >> 5) * 8, 0, SC1); }
    pin();
    bar();
    f32x4 t[8];
#pragma unroll
    for (int i = 0; i < 8; ++i) t[i] = *(const f32x4*)(lds + L_ORED + i * 1024 + l * 16);
    const f32x4 a = ((t[0] * f[0] + t[1] * f[1]) + (t[2] * f[2] + t[3] * f[3])) + ((t[4] * f[4] + t[5] * f[5]) + (t[6] * f[6] + t[7] * f[7]));
    st128(ws, WS_OP + ((2 * kvh + (l >> 5)) * 16 + sp) * 512 + (l & 15) * 32 + ((l >> 4) & 1) * 16, nosent4(a));
    pin();
    STAMP(5);
    if (role < 2) bar();
    if (role == 0) {
      bar();
      f32x4 r[4];
#pragma unroll
      for (int i = 0; i < 4; ++i) r[i] = *(const f32x4*)(lds + L_RED + ((l >> 4) * 4 + i) * 256 + (l & 15) * 16);
      f32x4 s = (r[0] + r[1]) + (r[2] + r[3]);
#pragma unroll
      for (int i = 0; i < 4; ++i) s[i] = x16_sum(s[i]);
      if (l < 16) st128(ws, WS_WP + (xg * D + 64 * idx) * 4 + l * 16, nosent4(s));
      STAMP(6);
    }
  }
  STAMP_DUMP();
}

extern "C" void kernel_launch(void* const* d_in, const int* in_sizes, int n_in, void* d_out, int out_size, void* d_ws, size_t ws_size,
                              hipStream_t stream) {
  fused_k<<<256, 640, 0, stream>>>(d_in[0], d_in[1], d_in[2], d_in[3], d_in[4], d_in[6], d_in[7], d_in[8], d_in[9], d_out, d_ws);
}
```

```cpp
#include <hip/hip_runtime.h>

__global__ void __launch_bounds__(576, 3) fused_k(const void* p_wm, unsigned n_wm, unsigned f_wm, const void* p_c, unsigned n_c, unsigned f_c, const void* p_k, unsigned n_k, unsigned f_k, const void* p_v, unsigned n_v, unsigned f_v, const void* p_ws, unsigned n_ws, unsigned f_ws, const void* p_wq, unsigned n_wq, unsigned f_wq, const void* p_x, unsigned n_x, unsigned f_x, const void* p_out, unsigned n_out, unsigned f_out, const void* p_wo, unsigned n_wo, unsigned f_wo, const void* p_w1, unsigned n_w1, unsigned f_w1, const void* p_w2, unsigned n_w2, unsigned f_w2) {
  __shared__ __attribute__((aligned(16))) unsigned char lds[96768];
  asm volatile(
    ".LSTART_2_%=:\n"
    "s_memrealtime s[62:63]\n"
    "s_load_dwordx16 s[4:19], s[0:1], 0x0\n"
    "s_load_dwordx16 s[20:35], s[0:1], 0x40\n"
    "s_load_dwordx8 s[36:43], s[0:1], 0x80\n"
    "s_load_dwordx4 s[44:47], s[0:1], 0xa0\n"
    "v_readfirstlane_b32 s48, v0\n"
    "v_and_b32 v1, 63, v0\n"
    "v_mov_b32 v2, 0\n"
    "v_mov_b32 v3, 0\n"
    "v_lshlrev_b32 v125, 3, v1\n"
    "ds_write_b64 v125, v[2:3] offset:640\n"
    "s_and_b32 s49, s2, 7\n"
    "s_lshr_b32 s50, s2, 3\n"
    "s_lshr_b32 s48, s48, 6\n"
    "s_cmp_lg_u32 s48, 8\n"
    "s_cbranch_scc1 .Lnopf_7_%=\n"
    "s_getpc_b64 s[84:85]\n"
    ".Lhere_8_%=:\n"
    "s_sub_u32 s84, s84, .Lhere_8_%=-.LSTART_2_%=\n"
    "s_subb_u32 s85, s85, 0\n"
    "s_and_b32 s85, s85, 0xffff\n"
    "s_mov_b32 s86, .LEND_%=-.LSTART_2_%=\n"
    "s_mov_b32 s87, 0x20000\n"
    "v_lshlrev_b32 v12, 12, v1\n"
    "s_lshl_b32 s57, s50, 7\n"
    "s_mov_b64 exec, 0x1f\n"
    "buffer_load_dword v166, v12, s[84:87], s57 offen\n"
    "s_mov_b64 exec, -1\n"
    ".Lnopf_7_%=:\n"
    "s_and_b32 s57, s50, 1\n"
    "s_lshl1_add_u32 s51, s49, s57\n"
    "s_lshr_b32 s52, s50, 1\n"
    "s_mul_i32 s64, s48, 2\n"
    "s_add_u32 s64, s64, s52\n"
    "s_and_b32 s64, s64, 15\n"
    "s_lshl4_add_u32 s64, s51, s64\n"
    "s_lshl_b32 s64, s64, 5\n"
    "s_add_u32 s64, s64, s48\n"
    "s_lshl_b32 s64, s64, 12\n"
    "s_add_u32 s65, s64, 0x8000\n"
    "s_add_u32 s66, s64, 0x10000\n"
    "s_add_u32 s67, s64, 0x18000\n"
    "v_lshrrev_b32 v8, 5, v1\n"
    "v_bfe_u32 v9, v1, 3, 1\n"
    "v_bfe_u32 v10, v1, 4, 1\n"
    "v_and_b32 v11, 7, v1\n"
    "v_lshrrev_b32 v14, 3, v1\n"
    "v_bfe_u32 v5, v1, 1, 1\n"
    "v_lshlrev_b32 v5, 11, v5\n"
    "v_bfe_u32 v13, v1, 2, 1\n"
    "v_lshl_add_u32 v5, v13, 9, v5\n"
    "v_and_b32 v6, 1, v1\n"
    "v_lshl_add_u32 v5, v6, 8, v5\n"
    "v_lshl_add_u32 v5, v9, 6, v5\n"
    "v_and_b32 v6, 48, v1\n"
    "v_add_u32 v5, v5, v6\n"
    "v_lshlrev_b32 v6, 4, v1\n"
    "s_cmp_gt_u32 s2, 71\n"
    "s_cbranch_scc1 .Lrole_3_%=\n"
    "s_mov_b32 s53, 0\n"
    "s_mov_b32 s95, 0\n"
    "s_mov_b32 s55, 771\n"
    "s_and_b32 s61, s2, 3\n"
    "s_mul_i32 s61, s61, 144\n"
    "s_lshl4_add_u32 s61, s48, s61\n"
    "s_lshr_b32 s57, s2, 2\n"
    "s_lshl_b32 s57, s57, 7\n"
    "s_mul_i32 s69, s61, 6912\n"
    "s_add_u32 s69, s69, s57\n"
    "s_add_u32 s70, s69, 55296\n"
    "s_lshl_b32 s71, s61, 1\n"
    "v_lshlrev_b32 v12, 2, v8\n"
    "v_lshl_add_u32 v12, v9, 1, v12\n"
    "v_add_u32 v12, v12, v10\n"
    "v_mul_u32_u24 v13, 6912, v12\n"
    "v_lshl_add_u32 v13, v11, 4, v13\n"
    "v_lshlrev_b32 v12, 2, v14\n"
    "s_waitcnt lgkmcnt(0)\n"
    "s_mov_b32 s3, s62\n"
    "s_memrealtime s[62:63]\n"
    "buffer_load_dword v160, v12, s[8:11], s71 offen\n"
    "buffer_load_dwordx4 v[136:139], v13, s[4:7], s69 offen nt\n"
    "buffer_load_dwordx4 v[140:143], v13, s[4:7], s70 offen nt\n"
    "v_lshlrev_b32 v167, 5, v9\n"
    "v_lshl_add_u32 v167, v10, 2, v167\n"
    "v_add_u32 v167, v167, v8\n"
    "v_mul_u32_u24 v167, 1152, v167\n"
    "v_lshl_add_u32 v167, v11, 4, v167\n"
    "s_lshl3_add_u32 s72, s49, s48\n"
    "s_mul_i32 s72, s72, 73728\n"
    "s_lshl_b32 s57, s50, 7\n"
    "s_add_u32 s72, s72, s57\n"
    "s_lshl_b32 s57, s48, 13\n"
    "s_add_u32 m0, s57, 23040\n"
    "s_branch .Lrole_6_%=\n"
    ".Lrole_3_%=:\n"
    "s_waitcnt lgkmcnt(0)\n"
    "s_mov_b32 s3, s62\n"
    "s_memrealtime s[62:63]\n"
    "s_cmp_eq_u32 s48, 8\n"
    "s_cbranch_scc1 .Lnk_9_%=\n"
    "s_sub_u32 s57, s2, 244\n"
    "s_cmp_lt_u32 s57, 3\n"
    "s_cbranch_scc1 .Lnk_9_%=\n"
    "buffer_load_dwordx4 v[16:19], v5, s[12:15], s64 offen nt\n"
    "buffer_load_dwordx4 v[20:23], v5, s[12:15], s64 offen offset:128 nt\n"
    ".Lnk_9_%=:\n"
    "s_cmp_gt_u32 s2, 143\n"
    "s_cbranch_scc1 .Lrole_4_%=\n"
    "s_mov_b32 s53, 1\n"
    "s_mov_b32 s95, 0\n"
    "s_mov_b32 s55, 771\n"
    "s_mul_i32 s56, s49, 9\n"
    "s_add_u32 s56, s56, s50\n"
    "s_sub_u32 s56, s56, 9\n"
    "s_lshl_b32 s72, s56, 7\n"
    "s_mov_b32 s73, 9216\n"
    "s_mov_b64 s[80:81], s[40:41]\n"
    "s_mov_b64 s[82:83], s[42:43]\n"
    "s_branch .Lrole_5_%=\n"
    ".Lrole_4_%=:\n"
    "s_lshr_b32 s61, s24, 7\n"
    "s_sub_u32 s61, 3, s61\n"
    "s_and_b32 s61, s61, 7\n"
    "s_sub_u32 s57, s50, 18\n"
    "s_cmp_eq_u32 s57, s61\n"
    "s_cselect_b32 s95, 1, 0\n"
    "s_cmp_eq_u32 s50, 31\n"
    "s_cselect_b32 s95, 2, s95\n"
    "s_cselect_b32 s57, s61, s57\n"
    "s_cselect_b32 s72, 2359296, 0\n"
    "s_cmp_lt_u32 s57, 8\n"
    "s_cbranch_scc1 .Lrq_10_%=\n"
    "s_mov_b32 s53, 3\n"
    "s_mov_b32 s55, 771\n"
    "s_sub_u32 s56, s2, 208\n"
    "s_lshl_b32 s72, s56, 7\n"
    "s_add_u32 s72, s72, 2304\n"
    "s_mov_b32 s73, 6912\n"
    "s_mov_b64 s[80:81], s[4:5]\n"
    "s_mov_b64 s[82:83], s[6:7]\n"
    "s_cmp_lt_u32 s2, 244\n"
    "s_cbranch_scc1 .Lrole_5_%=\n"
    "s_mov_b32 s55, 771\n"
    "s_cmp_lt_u32 s2, 247\n"
    "s_cselect_b32 s53, 4, 5\n"
    "s_cselect_b32 s55, 1028, s55\n"
    "s_branch .Lrole_5_%=\n"
    ".Lrq_10_%=:\n"
    "s_mov_b32 s53, 2\n"
    "s_mov_b32 s55, 771\n"
    "s_lshl3_add_u32 s56, s49, s57\n"
    "s_lshl_b32 s57, s56, 7\n"
    "s_add_u32 s72, s72, s57\n"
    "s_mov_b32 s73, 8192\n"
    "s_mov_b64 s[80:81], s[24:25]\n"
    "s_mov_b64 s[82:83], s[26:27]\n"
    ".Lrole_5_%=:\n"
    "s_lshl_b32 s57, s48, 3\n"
    "s_mul_i32 s57, s57, s73\n"
    "s_add_u32 s72, s72, s57\n"
    "v_mul_u32_u24 v167, s73, v14\n"
    "v_lshl_add_u32 v167, v11, 4, v167\n"
    "s_lshl_b32 s73, s73, 6\n"
    "s_lshl_b32 s57, s48, 10\n"
    "s_add_u32 m0, s57, 23040\n"
    ".Lrole_6_%=:\n"
    "s_cmp_eq_u32 s53, 4\n"
    "s_waitcnt lgkmcnt(0)\n"
    "s_barrier\n"
    "s_cbranch_scc1 .LH_11_%=\n"
    ".LHb_12_%=:\n"
    "s_mov_b32 s63, s62\n"
    "s_cmp_eq_u32 s48, 8\n"
    "s_cbranch_scc1 .LS_1_%=\n"
    "s_cmp_lg_u32 s53, 1\n"
    "s_cbranch_scc1 .Lnores_13_%=\n"
    "s_add_u32 s61, s80, s72\n"
    "s_bfe_u32 s61, s61, 0x30007\n"
    "s_cmp_eq_u32 s61, 3\n"
    "s_cselect_b32 s55, 1028, s55\n"
    "s_cselect_b32 s95, 0, 0\n"
    ".Lnores_13_%=:\n"
    "s_mov_b32 s68, 0\n"
    "s_and_b32 s55, s55, 255\n"
    "s_cmp_eq_u32 s53, 0\n"
    "s_cbranch_scc1 .Lw0_16_%=\n"
    "s_cmp_gt_u32 s53, 3\n"
    "s_cbranch_scc1 .Lnopre_14_%=\n"
    "s_cmp_lg_u32 s95, 0\n"
    "s_cselect_b32 s74, 5, 9\n"
    "s_sub_u32 s75, s48, s74\n"
    "s_cselect_b32 s75, s48, s75\n"
    "s_mul_i32 s57, s75, s73\n"
    "s_add_u32 s72, s72, s57\n"
    "s_lshl_b32 s57, s75, 13\n"
    "s_add_u32 m0, m0, s57\n"
    "s_sub_u32 s75, s74, s75\n"
    "s_mul_i32 s76, s74, s73\n"
    "s_lshl_b32 s77, s74, 13\n"
    "s_cmp_gt_u32 s55, 3\n"
    "s_cselect_b64 vcc, -1, 0\n"
    "s_cmp_gt_u32 s55, 2\n"
    "s_cbranch_vccnz .Lw4_18_%=\n"
    "s_cbranch_scc1 .Lw3_17_%=\n"
    "s_waitcnt vmcnt(1)\n"
    ".Lw3_17_%=:\n"
    "s_waitcnt vmcnt(2)\n"
    ".Lw4_18_%=:\n"
    "s_waitcnt vmcnt(3)\n"
    "buffer_load_dwordx4 v167, s[80:83], s72 offen nt lds\n"
    "s_add_u32 m0, m0, 8192\n"
    "s_add_u32 s72, s72, s73\n"
    "s_cmp_eq_u32 s75, 1\n"
    "s_cselect_b32 s57, s76, 0\n"
    "s_cselect_b32 s61, s77, 0\n"
    "s_sub_u32 m0, m0, s61\n"
    "s_sub_u32 s72, s72, s57\n"
    "s_cmp_gt_u32 s55, 3\n"
    "s_cselect_b64 vcc, -1, 0\n"
    "s_cmp_gt_u32 s55, 2\n"
    "s_cbranch_vccnz .Lw4_20_%=\n"
    "s_cbranch_scc1 .Lw3_19_%=\n"
    "s_waitcnt vmcnt(1)\n"
    ".Lw3_19_%=:\n"
    "s_waitcnt vmcnt(2)\n"
    ".Lw4_20_%=:\n"
    "s_waitcnt vmcnt(3)\n"
    "s_cmp_lg_u32 s48, 0\n"
    "s_cbranch_scc1 .Lfd_21_%=\n"
    "s_waitcnt vmcnt(2)\n"
    "s_memrealtime s[90:91]\n"
    "v_mov_b32 v164, 0\n"
    "s_waitcnt lgkmcnt(0)\n"
    "s_sub_u32 s90, s90, 30\n"
    "s_or_b32 s90, s90, 1\n"
    "v_mov_b32 v165, s90\n"
    "ds_write2_b32 v164, v165, v165 offset0:170 offset1:173\n"
    ".Lfd_21_%=:\n"
    "buffer_load_dwordx4 v167, s[80:83], s72 offen nt lds\n"
    "s_add_u32 m0, m0, 8192\n"
    "s_add_u32 s72, s72, s73\n"
    "s_cmp_eq_u32 s75, 2\n"
    "s_cselect_b32 s57, s76, 0\n"
    "s_cselect_b32 s61, s77, 0\n"
    "s_sub_u32 m0, m0, s61\n"
    "s_sub_u32 s72, s72, s57\n"
    "s_cmp_gt_u32 s55, 3\n"
    "s_cselect_b64 vcc, -1, 0\n"
    "s_cmp_gt_u32 s55, 2\n"
    "s_cbranch_vccnz .Lw4_23_%=\n"
    "s_cbranch_scc1 .Lw3_22_%=\n"
    "s_waitcnt vmcnt(1)\n"
    ".Lw3_22_%=:\n"
    "s_waitcnt vmcnt(2)\n"
    ".Lw4_23_%=:\n"
    "s_waitcnt vmcnt(3)\n"
    "buffer_load_dwordx4 v167, s[80:83], s72 offen nt lds\n"
    "s_add_u32 m0, m0, 8192\n"
    "s_add_u32 s72, s72, s73\n"
    "s_cmp_eq_u32 s75, 3\n"
    "s_cselect_b32 s57, s76, 0\n"
    "s_cselect_b32 s61, s77, 0\n"
    "s_sub_u32 m0, m0, s61\n"
    "s_sub_u32 s72, s72, s57\n"
    "s_cmp_gt_u32 s55, 3\n"
    "s_cselect_b64 vcc, -1, 0\n"
    "s_cmp_gt_u32 s55, 2\n"
    "s_cbranch_vccnz .Lw4_25_%=\n"
    "s_cbranch_scc1 .Lw3_24_%=\n"
    "s_waitcnt vmcnt(1)\n"
    ".Lw3_24_%=:\n"
    "s_waitcnt vmcnt(2)\n"
    ".Lw4_25_%=:\n"
    "s_waitcnt vmcnt(3)\n"
    "buffer_load_dwordx4 v167, s[80:83], s72 offen nt lds\n"
    "s_add_u32 m0, m0, 8192\n"
    "s_add_u32 s72, s72, s73\n"
    "s_cmp_eq_u32 s75, 4\n"
    "s_cselect_b32 s57, s76, 0\n"
    "s_cselect_b32 s61, s77, 0\n"
    "s_sub_u32 m0, m0, s61\n"
    "s_sub_u32 s72, s72, s57\n"
    "s_cmp_gt_u32 s55, 3\n"
    "s_cselect_b64 vcc, -1, 0\n"
    "s_cmp_gt_u32 s55, 2\n"
    "s_cbranch_vccnz .Lw4_27_%=\n"
    "s_cbranch_scc1 .Lw3_26_%=\n"
    "s_waitcnt vmcnt(1)\n"
    ".Lw3_26_%=:\n"
    "s_waitcnt vmcnt(2)\n"
    ".Lw4_27_%=:\n"
    "s_waitcnt vmcnt(3)\n"
    "buffer_load_dwordx4 v167, s[80:83], s72 offen nt lds\n"
    "s_add_u32 m0, m0, 8192\n"
    "s_add_u32 s72, s72, s73\n"
    "s_cmp_eq_u32 s75, 5\n"
    "s_cselect_b32 s57, s76, 0\n"
    "s_cselect_b32 s61, s77, 0\n"
    "s_sub_u32 m0, m0, s61\n"
    "s_sub_u32 s72, s72, s57\n"
    "s_cmp_lg_u32 s95, 0\n"
    "s_cbranch_scc1 .Lhalf_15_%=\n"
    "s_cmp_gt_u32 s55, 3\n"
    "s_cselect_b64 vcc, -1, 0\n"
    "s_cmp_gt_u32 s55, 2\n"
    "s_cbranch_vccnz .Lw4_29_%=\n"
    "s_cbranch_scc1 .Lw3_28_%=\n"
    "s_waitcnt vmcnt(1)\n"
    ".Lw3_28_%=:\n"
    "s_waitcnt vmcnt(2)\n"
    ".Lw4_29_%=:\n"
    "s_waitcnt vmcnt(3)\n"
    "buffer_load_dwordx4 v167, s[80:83], s72 offen nt lds\n"
    "s_add_u32 m0, m0, 8192\n"
    "s_add_u32 s72, s72, s73\n"
    "s_cmp_eq_u32 s75, 6\n"
    "s_cselect_b32 s57, s76, 0\n"
    "s_cselect_b32 s61, s77, 0\n"
    "s_sub_u32 m0, m0, s61\n"
    "s_sub_u32 s72, s72, s57\n"
    "s_cmp_gt_u32 s55, 3\n"
    "s_cselect_b64 vcc, -1, 0\n"
    "s_cmp_gt_u32 s55, 2\n"
    "s_cbranch_vccnz .Lw4_31_%=\n"
    "s_cbranch_scc1 .Lw3_30_%=\n"
    "s_waitcnt vmcnt(1)\n"
    ".Lw3_30_%=:\n"
    "s_waitcnt vmcnt(2)\n"
    ".Lw4_31_%=:\n"
    "s_waitcnt vmcnt(3)\n"
    "buffer_load_dwordx4 v167, s[80:83], s72 offen nt lds\n"
    "s_add_u32 m0, m0, 8192\n"
    "s_add_u32 s72, s72, s73\n"
    "s_cmp_eq_u32 s75, 7\n"
    "s_cselect_b32 s57, s76, 0\n"
    "s_cselect_b32 s61, s77, 0\n"
    "s_sub_u32 m0, m0, s61\n"
    "s_sub_u32 s72, s72, s57\n"
    "s_cmp_gt_u32 s55, 3\n"
    "s_cselect_b64 vcc, -1, 0\n"
    "s_cmp_gt_u32 s55, 2\n"
    "s_cbranch_vccnz .Lw4_33_%=\n"
    "s_cbranch_scc1 .Lw3_32_%=\n"
    "s_waitcnt vmcnt(1)\n"
    ".Lw3_32_%=:\n"
    "s_waitcnt vmcnt(2)\n"
    ".Lw4_33_%=:\n"
    "s_waitcnt vmcnt(3)\n"
    "buffer_load_dwordx4 v167, s[80:83], s72 offen nt lds\n"
    "s_add_u32 m0, m0, 8192\n"
    "s_add_u32 s72, s72, s73\n"
    "s_cmp_eq_u32 s75, 8\n"
    "s_cselect_b32 s57, s76, 0\n"
    "s_cselect_b32 s61, s77, 0\n"
    "s_sub_u32 m0, m0, s61\n"
    "s_sub_u32 s72, s72, s57\n"
    "s_cmp_gt_u32 s55, 3\n"
    "s_cselect_b64 vcc, -1, 0\n"
    "s_cmp_gt_u32 s55, 2\n"
    "s_cbranch_vccnz .Lw4_35_%=\n"
    "s_cbranch_scc1 .Lw3_34_%=\n"
    "s_waitcnt vmcnt(1)\n"
    ".Lw3_34_%=:\n"
    "s_waitcnt vmcnt(2)\n"
    ".Lw4_35_%=:\n"
    "s_waitcnt vmcnt(3)\n"
    "buffer_load_dwordx4 v167, s[80:83], s72 offen nt lds\n"
    "s_add_u32 m0, m0, 8192\n"
    "s_add_u32 s72, s72, s73\n"
    "s_branch .Lhalf_15_%=\n"
    ".Lw0_16_%=:\n"
    "buffer_load_dwordx4 v[16:19], v5, s[12:15], s64 offen nt\n"
    "buffer_load_dwordx4 v[20:23], v5, s[12:15], s64 offen offset:128 nt\n"
    "v_bfe_u32 v13, v1, 2, 1\n"
    "v_mul_u32_u24 v3, 0xffff, v13\n"
    "v_xor_b32 v2, 0xffff, v3\n"
    "v_and_b32 v13, 3, v1\n"
    "v_mul_u32_u24 v4, 0x5555, v13\n"
    "s_mov_b32 s58, 0x00ff00ff\n"
    "s_mov_b32 s59, 0xff00ff00\n"
    "v_lshlrev_b32 v7, 8, v8\n"
    "v_lshl_add_u32 v7, v11, 5, v7\n"
    "v_lshl_add_u32 v7, v10, 4, v7\n"
    "s_lshl_b32 s57, s48, 9\n"
    "v_add_u32 v7, s57, v7\n"
    "s_waitcnt vmcnt(4)\n"
    "s_cmp_lg_u32 s48, 0\n"
    "s_cbranch_scc1 .Lfd_36_%=\n"
    "s_memrealtime s[90:91]\n"
    "v_mov_b32 v164, 0\n"
    "s_waitcnt lgkmcnt(0)\n"
    "s_sub_u32 s90, s90, 30\n"
    "s_or_b32 s90, s90, 1\n"
    "v_mov_b32 v165, s90\n"
    "ds_write2_b32 v164, v165, v165 offset0:170 offset1:173\n"
    ".Lfd_36_%=:\n"
    "v_lshlrev_b32 v161, 16, v160\n"
    "v_and_b32 v160, 0xffff0000, v160\n"
    "v_mul_f32 v162, 0xbfb8aa3b, v161\n"
    "v_exp_f32 v162, v162\n"
    "s_nop 0\n"
    "v_add_f32 v162, 1.0, v162\n"
    "v_min_f32 v162, 0x7f000000, v162\n"
    "v_rcp_f32 v163, v162\n"
    "s_nop 0\n"
    "v_mul_f32 v13, v161, v163\n"
    "v_fma_f32 v161, -v13, v162, v161\n"
    "v_fma_f32 v161, v161, v163, v13\n"
    "v_mul_f32 v162, 0xbfb8aa3b, v160\n"
    "v_exp_f32 v162, v162\n"
    "s_nop 0\n"
    "v_add_f32 v162, 1.0, v162\n"
    "v_min_f32 v162, 0x7f000000, v162\n"
    "v_rcp_f32 v163, v162\n"
    "s_nop 0\n"
    "v_mul_f32 v13, v160, v163\n"
    "v_fma_f32 v160, -v13, v162, v160\n"
    "v_fma_f32 v160, v160, v163, v13\n"
    "v_cvt_pk_bf16_f32 v161, v161, v161\n"
    "v_cvt_pk_bf16_f32 v160, v160, v160\n"
    "v_mov_b32 v152, 0\n"
    "v_mov_b32 v153, 0\n"
    "v_mov_b32 v154, 0\n"
    "v_mov_b32 v155, 0\n"
    "v_and_b32 v156, v161, v2\n"
    "v_and_b32 v157, v161, v3\n"
    "v_and_b32 v158, v160, v2\n"
    "v_and_b32 v159, v160, v3\n"
    "v_mov_b32 v12, 0\n"
    "v_mov_b32 v13, 1\n"
    "s_waitcnt vmcnt(2)\n"
    "v_smfmac_f32_16x16x64_bf16 v[152:155], v[156:159], v[136:143], v4\n"
    "s_mov_b64 exec, s[58:59]\n"
    "s_nop 6\n"
    "ds_write_b128 v7, v[152:155] offset:9472\n"
    "s_mov_b64 exec, 1\n"
    "ds_add_u32 v12, v13 offset:648\n"
    "s_mov_b64 exec, -1\n"
    "s_cmp_gt_u32 s55, 3\n"
    "s_cselect_b64 vcc, -1, 0\n"
    "s_cmp_gt_u32 s55, 2\n"
    "s_cbranch_vccnz .Lw4_38_%=\n"
    "s_cbranch_scc1 .Lw3_37_%=\n"
    "s_waitcnt vmcnt(1)\n"
    ".Lw3_37_%=:\n"
    "s_waitcnt vmcnt(2)\n"
    ".Lw4_38_%=:\n"
    "s_waitcnt vmcnt(3)\n"
    "buffer_load_dwordx4 v167, s[36:39], s72 offen nt lds\n"
    "s_add_u32 m0, m0, 1024\n"
    "s_add_u32 s72, s72, 2304\n"
    "s_cmp_gt_u32 s55, 3\n"
    "s_cselect_b64 vcc, -1, 0\n"
    "s_cmp_gt_u32 s55, 2\n"
    "s_cbranch_vccnz .Lw4_40_%=\n"
    "s_cbranch_scc1 .Lw3_39_%=\n"
    "s_waitcnt vmcnt(1)\n"
    ".Lw3_39_%=:\n"
    "s_waitcnt vmcnt(2)\n"
    ".Lw4_40_%=:\n"
    "s_waitcnt vmcnt(3)\n"
    "buffer_load_dwordx4 v167, s[36:39], s72 offen nt lds\n"
    "s_add_u32 m0, m0, 1024\n"
    "s_add_u32 s72, s72, 6912\n"
    "s_cmp_gt_u32 s55, 3\n"
    "s_cselect_b64 vcc, -1, 0\n"
    "s_cmp_gt_u32 s55, 2\n"
    "s_cbranch_vccnz .Lw4_42_%=\n"
    "s_cbranch_scc1 .Lw3_41_%=\n"
    "s_waitcnt vmcnt(1)\n"
    ".Lw3_41_%=:\n"
    "s_waitcnt vmcnt(2)\n"
    ".Lw4_42_%=:\n"
    "s_waitcnt vmcnt(3)\n"
    "buffer_load_dwordx4 v167, s[36:39], s72 offen nt lds\n"
    "s_add_u32 m0, m0, 1024\n"
    "s_add_u32 s72, s72, 2304\n"
    "s_cmp_gt_u32 s55, 3\n"
    "s_cselect_b64 vcc, -1, 0\n"
    "s_cmp_gt_u32 s55, 2\n"
    "s_cbranch_vccnz .Lw4_44_%=\n"
    "s_cbranch_scc1 .Lw3_43_%=\n"
    "s_waitcnt vmcnt(1)\n"
    ".Lw3_43_%=:\n"
    "s_waitcnt vmcnt(2)\n"
    ".Lw4_44_%=:\n"
    "s_waitcnt vmcnt(3)\n"
    "buffer_load_dwordx4 v167, s[36:39], s72 offen nt lds\n"
    "s_add_u32 m0, m0, 1024\n"
    "s_add_u32 s72, s72, 6912\n"
    "s_cmp_gt_u32 s55, 3\n"
    "s_cselect_b64 vcc, -1, 0\n"
    "s_cmp_gt_u32 s55, 2\n"
    "s_cbranch_vccnz .Lw4_46_%=\n"
    "s_cbranch_scc1 .Lw3_45_%=\n"
    "s_waitcnt vmcnt(1)\n"
    ".Lw3_45_%=:\n"
    "s_waitcnt vmcnt(2)\n"
    ".Lw4_46_%=:\n"
    "s_waitcnt vmcnt(3)\n"
    "buffer_load_dwordx4 v167, s[36:39], s72 offen nt lds\n"
    "s_add_u32 m0, m0, 1024\n"
    "s_add_u32 s72, s72, 2304\n"
    "s_cmp_gt_u32 s55, 3\n"
    "s_cselect_b64 vcc, -1, 0\n"
    "s_cmp_gt_u32 s55, 2\n"
    "s_cbranch_vccnz .Lw4_48_%=\n"
    "s_cbranch_scc1 .Lw3_47_%=\n"
    "s_waitcnt vmcnt(1)\n"
    ".Lw3_47_%=:\n"
    "s_waitcnt vmcnt(2)\n"
    ".Lw4_48_%=:\n"
    "s_waitcnt vmcnt(3)\n"
    "buffer_load_dwordx4 v167, s[36:39], s72 offen nt lds\n"
    "s_add_u32 m0, m0, 1024\n"
    "s_add_u32 s72, s72, 6912\n"
    "s_cmp_gt_u32 s55, 3\n"
    "s_cselect_b64 vcc, -1, 0\n"
    "s_cmp_gt_u32 s55, 2\n"
    "s_cbranch_vccnz .Lw4_50_%=\n"
    "s_cbranch_scc1 .Lw3_49_%=\n"
    "s_waitcnt vmcnt(1)\n"
    ".Lw3_49_%=:\n"
    "s_waitcnt vmcnt(2)\n"
    ".Lw4_50_%=:\n"
    "s_waitcnt vmcnt(3)\n"
    "buffer_load_dwordx4 v167, s[36:39], s72 offen nt lds\n"
    "s_add_u32 m0, m0, 1024\n"
    "s_add_u32 s72, s72, 2304\n"
    "s_cmp_gt_u32 s55, 3\n"
    "s_cselect_b64 vcc, -1, 0\n"
    "s_cmp_gt_u32 s55, 2\n"
    "s_cbranch_vccnz .Lw4_52_%=\n"
    "s_cbranch_scc1 .Lw3_51_%=\n"
    "s_waitcnt vmcnt(1)\n"
    ".Lw3_51_%=:\n"
    "s_waitcnt vmcnt(2)\n"
    ".Lw4_52_%=:\n"
    "s_waitcnt vmcnt(3)\n"
    "buffer_load_dwordx4 v167, s[36:39], s72 offen nt lds\n"
    "s_add_u32 m0, m0, 1024\n"
    "s_add_u32 s72, s72, 6912\n"
    "s_branch .Lhalf_15_%=\n"
    ".Lnopre_14_%=:\n"
    "s_cmp_lg_u32 s53, 4\n"
    "s_cbranch_scc1 .Lr5_53_%=\n"
    "buffer_load_dwordx4 v[16:19], v5, s[12:15], s64 offen nt\n"
    "buffer_load_dwordx4 v[20:23], v5, s[12:15], s64 offen offset:128 nt\n"
    "s_branch .Lhalf_15_%=\n"
    ".Lr5_53_%=:\n"
    "s_cmp_lg_u32 s48, 0\n"
    "s_cbranch_scc1 .Lfd_54_%=\n"
    "s_waitcnt vmcnt(1)\n"
    "s_memrealtime s[90:91]\n"
    "v_mov_b32 v164, 0\n"
    "s_waitcnt lgkmcnt(0)\n"
    "s_sub_u32 s90, s90, 30\n"
    "s_or_b32 s90, s90, 1\n"
    "v_mov_b32 v165, s90\n"
    "ds_write2_b32 v164, v165, v165 offset0:170 offset1:173\n"
    ".Lfd_54_%=:\n"
    ".Lhalf_15_%=:\n"
    "v_bfe_u32 v13, v1, 2, 1\n"
    "v_mul_u32_u24 v3, 0xffff, v13\n"
    "v_xor_b32 v2, 0xffff, v3\n"
    "v_and_b32 v13, 3, v1\n"
    "v_mul_u32_u24 v4, 0x5555, v13\n"
    "s_mov_b32 s58, 0x00ff00ff\n"
    "s_mov_b32 s59, 0xff00ff00\n"
    "v_lshlrev_b32 v7, 8, v8\n"
    "v_lshl_add_u32 v7, v11, 5, v7\n"
    "v_lshl_add_u32 v7, v10, 4, v7\n"
    "s_lshl_b32 s57, s48, 9\n"
    "v_add_u32 v7, s57, v7\n"
    "v_mov_b32 v12, 0\n"
    "v_mov_b32 v13, 1\n"
    "s_waitcnt lgkmcnt(0)\n"
    "s_cmp_gt_u32 s55, 3\n"
    "s_cselect_b64 vcc, -1, 0\n"
    "s_cmp_gt_u32 s55, 2\n"
    "s_cbranch_vccnz .Lw4_56_%=\n"
    "s_cbranch_scc1 .Lw3_55_%=\n"
    "s_waitcnt vmcnt(1)\n"
    ".Lw3_55_%=:\n"
    "s_waitcnt vmcnt(2)\n"
    ".Lw4_56_%=:\n"
    "s_waitcnt vmcnt(3)\n"
    "s_memrealtime s[90:91]\n"
    "s_cmp_eq_u32 s53, 1\n"
    "s_cselect_b32 s57, 70, 0\n"
    "s_waitcnt lgkmcnt(0)\n"
    "s_sub_u32 s90, s90, s63\n"
    "s_add_u32 s90, s90, s57\n"
    "s_cmp_gt_u32 s90, 329\n"
    "s_cselect_b32 s55, 4, 3\n"
    "s_cmp_lt_u32 s90, 294\n"
    "s_cselect_b32 s55, 2, s55\n"
    "s_cmp_gt_u32 s55, 3\n"
    "s_cselect_b64 vcc, -1, 0\n"
    "s_cmp_gt_u32 s55, 2\n"
    "buffer_load_dwordx4 v[24:27], v5, s[12:15], s64 offen offset:1024 nt\n"
    "s_cbranch_vccnz .Lw4_58_%=\n"
    "s_cbranch_scc1 .Lw3_57_%=\n"
    "s_waitcnt vmcnt(1)\n"
    ".Lw3_57_%=:\n"
    "s_waitcnt vmcnt(2)\n"
    ".Lw4_58_%=:\n"
    "s_waitcnt vmcnt(3)\n"
    "ds_read_b64 v[164:165], v12 offset:680\n"
    "s_memrealtime s[90:91]\n"
    "s_cmp_eq_u32 s53, 1\n"
    "s_cselect_b32 s57, 70, 0\n"
    "s_waitcnt lgkmcnt(0)\n"
    "v_readfirstlane_b32 s91, v164\n"
    "v_readfirstlane_b32 s68, v165\n"
    "s_cmp_lg_u32 s91, 0\n"
    "s_cselect_b32 s63, s91, s63\n"
    "s_sub_u32 s90, s90, s63\n"
    "s_add_u32 s90, s90, s57\n"
    "s_cmp_gt_u32 s90, 358\n"
    "s_cselect_b32 s55, 4, 3\n"
    "s_cmp_lt_u32 s90, 323\n"
    "s_cselect_b32 s55, 2, s55\n"
    "s_cmp_gt_u32 s55, 3\n"
    "s_cselect_b64 vcc, -1, 0\n"
    "s_cmp_gt_u32 s55, 2\n"
    "buffer_load_dwordx4 v[28:31], v5, s[12:15], s64 offen offset:1152 nt\n"
    "s_cbranch_vccnz .Lw4_60_%=\n"
    "s_cbranch_scc1 .Lw3_59_%=\n"
    "s_waitcnt vmcnt(1)\n"
    ".Lw3_59_%=:\n"
    "s_waitcnt vmcnt(2)\n"
    ".Lw4_60_%=:\n"
    "s_waitcnt vmcnt(3)\n"
    "s_memrealtime s[90:91]\n"
    "s_cmp_eq_u32 s53, 1\n"
    "s_cselect_b32 s57, 70, 0\n"
    "s_waitcnt lgkmcnt(0)\n"
    "s_sub_u32 s90, s90, s63\n"
    "s_add_u32 s90, s90, s57\n"
    "s_cmp_gt_u32 s90, 388\n"
    "s_cselect_b32 s55, 4, 3\n"
    "s_cmp_lt_u32 s90, 353\n"
    "s_cselect_b32 s55, 2, s55\n"
    "s_cmp_gt_u32 s55, 3\n"
    "s_cselect_b64 vcc, -1, 0\n"
    "s_cmp_gt_u32 s55, 2\n"
    "buffer_load_dwordx4 v[32:35], v5, s[12:15], s65 offen nt\n"
    "s_cbranch_vccnz .Lw4_62_%=\n"
    "s_cbranch_scc1 .Lw3_61_%=\n"
    "s_waitcnt vmcnt(1)\n"
    ".Lw3_61_%=:\n"
    "s_waitcnt vmcnt(2)\n"
    ".Lw4_62_%=:\n"
    "s_waitcnt vmcnt(3)\n"
    "s_mov_b64 exec, 1\n"
    "ds_add_u32 v12, v13 offset:652\n"
    "s_mov_b64 exec, -1\n"
    "buffer_load_dwordx4 v[36:39], v5, s[12:15], s65 offen offset:128 nt\n"
    "s_cbranch_vccnz .Lw4_64_%=\n"
    "s_cbranch_scc1 .Lw3_63_%=\n"
    "s_waitcnt vmcnt(1)\n"
    ".Lw3_63_%=:\n"
    "s_waitcnt vmcnt(2)\n"
    ".Lw4_64_%=:\n"
    "s_waitcnt vmcnt(3)\n"
    "s_memrealtime s[90:91]\n"
    "s_cmp_eq_u32 s53, 1\n"
    "s_cselect_b32 s57, 70, 0\n"
    "s_waitcnt lgkmcnt(0)\n"
    "s_sub_u32 s90, s90, s63\n"
    "s_add_u32 s90, s90, s57\n"
    "s_cmp_gt_u32 s90, 447\n"
    "s_cselect_b32 s55, 4, 3\n"
    "s_cmp_lt_u32 s90, 412\n"
    "s_cselect_b32 s55, 2, s55\n"
    "s_cmp_gt_u32 s55, 3\n"
    "s_cselect_b64 vcc, -1, 0\n"
    "s_cmp_gt_u32 s55, 2\n"
    "buffer_load_dwordx4 v[40:43], v5, s[12:15], s65 offen offset:1024 nt\n"
    "s_cbranch_vccnz .Lw4_66_%=\n"
    "s_cbranch_scc1 .Lw3_65_%=\n"
    "s_waitcnt vmcnt(1)\n"
    ".Lw3_65_%=:\n"
    "s_waitcnt vmcnt(2)\n"
    ".Lw4_66_%=:\n"
    "s_waitcnt vmcnt(3)\n"
    "buffer_load_dwordx4 v[44:47], v5, s[12:15], s65 offen offset:1152 nt\n"
    "s_cbranch_vccnz .Lw4_68_%=\n"
    "s_cbranch_scc1 .Lw3_67_%=\n"
    "s_waitcnt vmcnt(1)\n"
    ".Lw3_67_%=:\n"
    "s_waitcnt vmcnt(2)\n"
    ".Lw4_68_%=:\n"
    "s_waitcnt vmcnt(3)\n"
    "s_memrealtime s[90:91]\n"
    "s_cmp_eq_u32 s53, 1\n"
    "s_cselect_b32 s57, 70, 0\n"
    "s_waitcnt lgkmcnt(0)\n"
    "s_sub_u32 s90, s90, s63\n"
    "s_add_u32 s90, s90, s57\n"
    "s_cmp_gt_u32 s90, 506\n"
    "s_cselect_b32 s55, 4, 3\n"
    "s_cmp_lt_u32 s90, 471\n"
    "s_cselect_b32 s55, 2, s55\n"
    "s_cmp_gt_u32 s55, 3\n"
    "s_cselect_b64 vcc, -1, 0\n"
    "s_cmp_gt_u32 s55, 2\n"
    "buffer_load_dwordx4 v[48:51], v5, s[12:15], s66 offen nt\n"
    "s_cbranch_vccnz .Lw4_70_%=\n"
    "s_cbranch_scc1 .Lw3_69_%=\n"
    "s_waitcnt vmcnt(1)\n"
    ".Lw3_69_%=:\n"
    "s_waitcnt vmcnt(2)\n"
    ".Lw4_70_%=:\n"
    "s_waitcnt vmcnt(3)\n"
    "buffer_load_dwordx4 v[52:55], v5, s[12:15], s66 offen offset:128 nt\n"
    "s_cbranch_vccnz .Lw4_72_%=\n"
    "s_cbranch_scc1 .Lw3_71_%=\n"
    "s_waitcnt vmcnt(1)\n"
    ".Lw3_71_%=:\n"
    "s_waitcnt vmcnt(2)\n"
    ".Lw4_72_%=:\n"
    "s_waitcnt vmcnt(3)\n"
    "s_memrealtime s[90:91]\n"
    "s_cmp_eq_u32 s53, 1\n"
    "s_cselect_b32 s57, 70, 0\n"
    "s_waitcnt lgkmcnt(0)\n"
    "s_sub_u32 s90, s90, s63\n"
    "s_add_u32 s90, s90, s57\n"
    "s_cmp_gt_u32 s90, 565\n"
    "s_cselect_b32 s55, 4, 3\n"
    "s_cmp_lt_u32 s90, 530\n"
    "s_cselect_b32 s55, 2, s55\n"
    "s_cmp_gt_u32 s55, 3\n"
    "s_cselect_b64 vcc, -1, 0\n"
    "s_cmp_gt_u32 s55, 2\n"
    "s_mov_b64 exec, 1\n"
    "ds_add_u32 v12, v13 offset:672\n"
    "s_mov_b64 exec, -1\n"
    "buffer_load_dwordx4 v[56:59], v5, s[12:15], s66 offen offset:1024 nt\n"
    "s_cbranch_vccnz .Lw4_74_%=\n"
    "s_cbranch_scc1 .Lw3_73_%=\n"
    "s_waitcnt vmcnt(1)\n"
    ".Lw3_73_%=:\n"
    "s_waitcnt vmcnt(2)\n"
    ".Lw4_74_%=:\n"
    "s_waitcnt vmcnt(3)\n"
    "buffer_load_dwordx4 v[60:63], v5, s[12:15], s66 offen offset:1152 nt\n"
    "s_cbranch_vccnz .Lw4_76_%=\n"
    "s_cbranch_scc1 .Lw3_75_%=\n"
    "s_waitcnt vmcnt(1)\n"
    ".Lw3_75_%=:\n"
    "s_waitcnt vmcnt(2)\n"
    ".Lw4_76_%=:\n"
    "s_waitcnt vmcnt(3)\n"
    "ds_read_b64 v[164:165], v12 offset:680\n"
    "s_memrealtime s[90:91]\n"
    "s_cmp_eq_u32 s53, 1\n"
    "s_cselect_b32 s57, 70, 0\n"
    "s_waitcnt lgkmcnt(0)\n"
    "v_readfirstlane_b32 s91, v164\n"
    "v_readfirstlane_b32 s68, v165\n"
    "s_cmp_lg_u32 s91, 0\n"
    "s_cselect_b32 s63, s91, s63\n"
    "s_sub_u32 s90, s90, s63\n"
    "s_add_u32 s90, s90, s57\n"
    "s_cmp_gt_u32 s90, 624\n"
    "s_cselect_b32 s55, 4, 3\n"
    "s_cmp_lt_u32 s90, 589\n"
    "s_cselect_b32 s55, 2, s55\n"
    "s_cmp_gt_u32 s55, 3\n"
    "s_cselect_b64 vcc, -1, 0\n"
    "s_cmp_gt_u32 s55, 2\n"
    "buffer_load_dwordx4 v[64:67], v5, s[12:15], s67 offen nt\n"
    "s_cbranch_vccnz .Lw4_78_%=\n"
    "s_cbranch_scc1 .Lw3_77_%=\n"
    "s_waitcnt vmcnt(1)\n"
    ".Lw3_77_%=:\n"
    "s_waitcnt vmcnt(2)\n"
    ".Lw4_78_%=:\n"
    "s_waitcnt vmcnt(3)\n"
    "buffer_load_dwordx4 v[68:71], v5, s[12:15], s67 offen offset:128 nt\n"
    "s_cbranch_vccnz .Lw4_80_%=\n"
    "s_cbranch_scc1 .Lw3_79_%=\n"
    "s_waitcnt vmcnt(1)\n"
    ".Lw3_79_%=:\n"
    "s_waitcnt vmcnt(2)\n"
    ".Lw4_80_%=:\n"
    "s_waitcnt vmcnt(3)\n"
    "ds_read_b64 v[164:165], v12 offset:680\n"
    "s_memrealtime s[90:91]\n"
    "s_cmp_eq_u32 s53, 1\n"
    "s_cselect_b32 s57, 70, 0\n"
    "s_waitcnt lgkmcnt(0)\n"
    "v_readfirstlane_b32 s91, v164\n"
    "v_readfirstlane_b32 s68, v165\n"
    "s_cmp_lg_u32 s91, 0\n"
    "s_cselect_b32 s63, s91, s63\n"
    "s_sub_u32 s90, s90, s63\n"
    "s_add_u32 s90, s90, s57\n"
    "s_cmp_gt_u32 s90, 683\n"
    "s_cselect_b32 s55, 4, 3\n"
    "s_cmp_lt_u32 s90, 648\n"
    "s_cselect_b32 s55, 2, s55\n"
    "s_cmp_gt_u32 s55, 3\n"
    "s_cselect_b64 vcc, -1, 0\n"
    "s_cmp_gt_u32 s55, 2\n"
    "buffer_load_dwordx4 v[72:75], v5, s[12:15], s67 offen offset:1024 nt\n"
    "s_cbranch_vccnz .Lw4_82_%=\n"
    "s_cbranch_scc1 .Lw3_81_%=\n"
    "s_waitcnt vmcnt(1)\n"
    ".Lw3_81_%=:\n"
    "s_waitcnt vmcnt(2)\n"
    ".Lw4_82_%=:\n"
    "s_waitcnt vmcnt(3)\n"
    "buffer_load_dwordx4 v[76:79], v5, s[12:15], s67 offen offset:1152 nt\n"
    "s_cbranch_vccnz .Lw4_84_%=\n"
    "s_cbranch_scc1 .Lw3_83_%=\n"
    "s_waitcnt vmcnt(1)\n"
    ".Lw3_83_%=:\n"
    "s_waitcnt vmcnt(2)\n"
    ".Lw4_84_%=:\n"
    "s_waitcnt vmcnt(3)\n"
    "ds_read_b64 v[164:165], v12 offset:680\n"
    "s_memrealtime s[90:91]\n"
    "s_cmp_eq_u32 s53, 1\n"
    "s_cselect_b32 s57, 70, 0\n"
    "s_waitcnt lgkmcnt(0)\n"
    "v_readfirstlane_b32 s91, v164\n"
    "v_readfirstlane_b32 s68, v165\n"
    "s_cmp_lg_u32 s91, 0\n"
    "s_cselect_b32 s63, s91, s63\n"
    "s_sub_u32 s90, s90, s63\n"
    "s_add_u32 s90, s90, s57\n"
    "s_cmp_gt_u32 s90, 742\n"
    "s_cselect_b32 s55, 4, 3\n"
    "s_cmp_lt_u32 s90, 707\n"
    "s_cselect_b32 s55, 2, s55\n"
    "s_cmp_gt_u32 s55, 3\n"
    "s_cselect_b64 vcc, -1, 0\n"
    "s_cmp_gt_u32 s55, 2\n"
    "buffer_load_dwordx4 v[80:83], v6, s[16:19], s64 offen nt\n"
    "s_cbranch_vccnz .Lw4_86_%=\n"
    "s_cbranch_scc1 .Lw3_85_%=\n"
    "s_waitcnt vmcnt(1)\n"
    ".Lw3_85_%=:\n"
    "s_waitcnt vmcnt(2)\n"
    ".Lw4_86_%=:\n"
    "s_waitcnt vmcnt(3)\n"
    "buffer_load_dwordx4 v[84:87], v6, s[16:19], s64 offen offset:1024 nt\n"
    "s_cbranch_vccnz .Lw4_88_%=\n"
    "s_cbranch_scc1 .Lw3_87_%=\n"
    "s_waitcnt vmcnt(1)\n"
    ".Lw3_87_%=:\n"
    "s_waitcnt vmcnt(2)\n"
    ".Lw4_88_%=:\n"
    "s_waitcnt vmcnt(3)\n"
    "ds_read_b64 v[164:165], v12 offset:680\n"
    "s_memrealtime s[90:91]\n"
    "s_cmp_eq_u32 s53, 1\n"
    "s_cselect_b32 s57, 70, 0\n"
    "s_waitcnt lgkmcnt(0)\n"
    "v_readfirstlane_b32 s91, v164\n"
    "v_readfirstlane_b32 s68, v165\n"
    "s_cmp_lg_u32 s91, 0\n"
    "s_cselect_b32 s63, s91, s63\n"
    "s_sub_u32 s90, s90, s63\n"
    "s_add_u32 s90, s90, s57\n"
    "s_cmp_gt_u32 s90, 801\n"
    "s_cselect_b32 s55, 4, 3\n"
    "s_cmp_lt_u32 s90, 766\n"
    "s_cselect_b32 s55, 2, s55\n"
    "s_cmp_gt_u32 s55, 3\n"
    "s_cselect_b64 vcc, -1, 0\n"
    "s_cmp_gt_u32 s55, 2\n"
    "buffer_load_dwordx4 v[88:91], v6, s[16:19], s64 offen offset:2048 nt\n"
    "s_cbranch_vccnz .Lw4_90_%=\n"
    "s_cbranch_scc1 .Lw3_89_%=\n"
    "s_waitcnt vmcnt(1)\n"
    ".Lw3_89_%=:\n"
    "s_waitcnt vmcnt(2)\n"
    ".Lw4_90_%=:\n"
    "s_waitcnt vmcnt(3)\n"
    "buffer_load_dwordx4 v[92:95], v6, s[16:19], s64 offen offset:3072 nt\n"
    "s_cbranch_vccnz .Lw4_92_%=\n"
    "s_cbranch_scc1 .Lw3_91_%=\n"
    "s_waitcnt vmcnt(1)\n"
    ".Lw3_91_%=:\n"
    "s_waitcnt vmcnt(2)\n"
    ".Lw4_92_%=:\n"
    "s_waitcnt vmcnt(3)\n"
    "ds_read_b64 v[164:165], v12 offset:680\n"
    "s_memrealtime s[90:91]\n"
    "s_cmp_eq_u32 s53, 1\n"
    "s_cselect_b32 s57, 70, 0\n"
    "s_waitcnt lgkmcnt(0)\n"
    "v_readfirstlane_b32 s91, v164\n"
    "v_readfirstlane_b32 s68, v165\n"
    "s_cmp_lg_u32 s91, 0\n"
    "s_cselect_b32 s63, s91, s63\n"
    "s_sub_u32 s90, s90, s63\n"
    "s_add_u32 s90, s90, s57\n"
    "s_cmp_gt_u32 s90, 860\n"
    "s_cselect_b32 s55, 4, 3\n"
    "s_cmp_lt_u32 s90, 825\n"
    "s_cselect_b32 s55, 2, s55\n"
    "s_cmp_gt_u32 s55, 3\n"
    "s_cselect_b64 vcc, -1, 0\n"
    "s_cmp_gt_u32 s55, 2\n"
    "buffer_load_dwordx4 v[96:99], v6, s[16:19], s65 offen nt\n"
    "s_cbranch_vccnz .Lw4_94_%=\n"
    "s_cbranch_scc1 .Lw3_93_%=\n"
    "s_waitcnt vmcnt(1)\n"
    ".Lw3_93_%=:\n"
    "s_waitcnt vmcnt(2)\n"
    ".Lw4_94_%=:\n"
    "s_waitcnt vmcnt(3)\n"
    "buffer_load_dwordx4 v[100:103], v6, s[16:19], s65 offen offset:1024 nt\n"
    "s_cbranch_vccnz .Lw4_96_%=\n"
    "s_cbranch_scc1 .Lw3_95_%=\n"
    "s_waitcnt vmcnt(1)\n"
    ".Lw3_95_%=:\n"
    "s_waitcnt vmcnt(2)\n"
    ".Lw4_96_%=:\n"
    "s_waitcnt vmcnt(3)\n"
    "ds_read_b64 v[164:165], v12 offset:680\n"
    "s_memrealtime s[90:91]\n"
    "s_cmp_eq_u32 s53, 1\n"
    "s_cselect_b32 s57, 70, 0\n"
    "s_waitcnt lgkmcnt(0)\n"
    "v_readfirstlane_b32 s91, v164\n"
    "v_readfirstlane_b32 s68, v165\n"
    "s_cmp_lg_u32 s91, 0\n"
    "s_cselect_b32 s63, s91, s63\n"
    "s_sub_u32 s90, s90, s63\n"
    "s_add_u32 s90, s90, s57\n"
    "s_cmp_gt_u32 s90, 919\n"
    "s_cselect_b32 s55, 4, 3\n"
    "s_cmp_lt_u32 s90, 884\n"
    "s_cselect_b32 s55, 2, s55\n"
    "s_cmp_gt_u32 s55, 3\n"
    "s_cselect_b64 vcc, -1, 0\n"
    "s_cmp_gt_u32 s55, 2\n"
    "buffer_load_dwordx4 v[104:107], v6, s[16:19], s65 offen offset:2048 nt\n"
    "s_cbranch_vccnz .Lw4_98_%=\n"
    "s_cbranch_scc1 .Lw3_97_%=\n"
    "s_waitcnt vmcnt(1)\n"
    ".Lw3_97_%=:\n"
    "s_waitcnt vmcnt(2)\n"
    ".Lw4_98_%=:\n"
    "s_waitcnt vmcnt(3)\n"
    "buffer_load_dwordx4 v[108:111], v6, s[16:19], s65 offen offset:3072 nt\n"
    "s_cbranch_vccnz .Lw4_100_%=\n"
    "s_cbranch_scc1 .Lw3_99_%=\n"
    "s_waitcnt vmcnt(1)\n"
    ".Lw3_99_%=:\n"
    "s_waitcnt vmcnt(2)\n"
    ".Lw4_100_%=:\n"
    "s_waitcnt vmcnt(3)\n"
    "ds_read_b64 v[164:165], v12 offset:680\n"
    "s_memrealtime s[90:91]\n"
    "s_cmp_eq_u32 s53, 1\n"
    "s_cselect_b32 s57, 70, 0\n"
    "s_waitcnt lgkmcnt(0)\n"
    "v_readfirstlane_b32 s91, v164\n"
    "v_readfirstlane_b32 s68, v165\n"
    "s_cmp_lg_u32 s91, 0\n"
    "s_cselect_b32 s63, s91, s63\n"
    "s_sub_u32 s90, s90, s63\n"
    "s_add_u32 s90, s90, s57\n"
    "s_cmp_gt_u32 s90, 978\n"
    "s_cselect_b32 s55, 4, 3\n"
    "s_cmp_lt_u32 s90, 943\n"
    "s_cselect_b32 s55, 2, s55\n"
    "s_cmp_gt_u32 s55, 3\n"
    "s_cselect_b64 vcc, -1, 0\n"
    "s_cmp_gt_u32 s55, 2\n"
    "buffer_load_dwordx4 v[112:115], v6, s[16:19], s66 offen nt\n"
    "s_cbranch_vccnz .Lw4_102_%=\n"
    "s_cbranch_scc1 .Lw3_101_%=\n"
    "s_waitcnt vmcnt(1)\n"
    ".Lw3_101_%=:\n"
    "s_waitcnt vmcnt(2)\n"
    ".Lw4_102_%=:\n"
    "s_waitcnt vmcnt(3)\n"
    "buffer_load_dwordx4 v[116:119], v6, s[16:19], s66 offen offset:1024 nt\n"
    "s_cbranch_vccnz .Lw4_104_%=\n"
    "s_cbranch_scc1 .Lw3_103_%=\n"
    "s_waitcnt vmcnt(1)\n"
    ".Lw3_103_%=:\n"
    "s_waitcnt vmcnt(2)\n"
    ".Lw4_104_%=:\n"
    "s_waitcnt vmcnt(3)\n"
    "ds_read_b64 v[164:165], v12 offset:680\n"
    "s_memrealtime s[90:91]\n"
    "s_cmp_eq_u32 s53, 1\n"
    "s_cselect_b32 s57, 70, 0\n"
    "s_waitcnt lgkmcnt(0)\n"
    "v_readfirstlane_b32 s91, v164\n"
    "v_readfirstlane_b32 s68, v165\n"
    "s_cmp_lg_u32 s91, 0\n"
    "s_cselect_b32 s63, s91, s63\n"
    "s_sub_u32 s90, s90, s63\n"
    "s_add_u32 s90, s90, s57\n"
    "s_cmp_gt_u32 s90, 1037\n"
    "s_cselect_b32 s55, 4, 3\n"
    "s_cmp_lt_u32 s90, 1002\n"
    "s_cselect_b32 s55, 2, s55\n"
    "s_cmp_gt_u32 s55, 3\n"
    "s_cselect_b64 vcc, -1, 0\n"
    "s_cmp_gt_u32 s55, 2\n"
    "buffer_load_dwordx4 v[120:123], v6, s[16:19], s66 offen offset:2048 nt\n"
    "s_cbranch_vccnz .Lw4_106_%=\n"
    "s_cbranch_scc1 .Lw3_105_%=\n"
    "s_waitcnt vmcnt(1)\n"
    ".Lw3_105_%=:\n"
    "s_waitcnt vmcnt(2)\n"
    ".Lw4_106_%=:\n"
    "s_waitcnt vmcnt(3)\n"
    "buffer_load_dwordx4 v[124:127], v6, s[16:19], s66 offen offset:3072 nt\n"
    "v_lshlrev_b32 v160, 8, v9\n"
    "v_and_b32 v13, 1, v1\n"
    "v_lshl_add_u32 v160, v13, 6, v160\n"
    "v_and_b32 v13, 48, v1\n"
    "v_add_u32 v160, v160, v13\n"
    "v_bfe_u32 v161, v1, 1, 2\n"
    "v_mov_b32 v162, 896\n"
    "v_cmp_eq_u32 vcc, 0, v161\n"
    "s_nop 1\n"
    "v_cndmask_b32 v163, v162, v160, vcc\n"
    "v_cmp_eq_u32 vcc, 1, v161\n"
    "s_nop 1\n"
    "v_cndmask_b32 v164, v162, v160, vcc\n"
    "v_cmp_eq_u32 vcc, 2, v161\n"
    "s_nop 1\n"
    "v_cndmask_b32 v0, v162, v160, vcc\n"
    "v_cmp_eq_u32 vcc, 3, v161\n"
    "s_nop 1\n"
    "v_cndmask_b32 v14, v162, v160, vcc\n"
    "s_mov_b32 s60, 4194304\n"
    "v_mov_b32 v12, 0\n"
    ".Llw_107_%=:\n"
    "ds_read_b32 v13, v12 offset:640\n"
    "s_waitcnt lgkmcnt(0)\n"
    "v_cmp_ne_u32 vcc, 0, v13\n"
    "s_cbranch_vccnz .Llwd_108_%=\n"
    "s_sleep 2\n"
    "s_sub_u32 s60, s60, 1\n"
    "s_cbranch_scc0 .Llw_107_%=\n"
    ".Llwd_108_%=:\n"
    "v_readfirstlane_b32 s54, v13\n"
    "ds_read_b32 v13, v12 offset:688\n"
    "s_waitcnt lgkmcnt(0)\n"
    "v_readfirstlane_b32 s63, v13\n"
    "ds_read_b128 v[144:147], v163\n"
    "ds_read_b128 v[148:151], v163 offset:128\n"
    "ds_read_b128 v[152:155], v164\n"
    "ds_read_b128 v[156:159], v164 offset:128\n"
    "s_waitcnt lgkmcnt(0)\n"
    "v_mfma_f32_16x16x32_bf16 v[160:163], v[16:19], v[144:147], 0\n"
    "v_mfma_f32_16x16x32_bf16 v[164:167], v[24:27], v[144:147], 0\n"
    "v_mfma_f32_16x16x32_bf16 v[160:163], v[20:23], v[148:151], v[160:163]\n"
    "v_mfma_f32_16x16x32_bf16 v[164:167], v[28:31], v[148:151], v[164:167]\n"
    "s_nop 3\n"
    "ds_read_b128 v[144:147], v0\n"
    "ds_read_b128 v[148:151], v0 offset:128\n"
    "s_waitcnt lgkmcnt(2)\n"
    "v_mfma_f32_16x16x32_bf16 v[160:163], v[32:35], v[152:155], v[160:163]\n"
    "v_mfma_f32_16x16x32_bf16 v[164:167], v[40:43], v[152:155], v[164:167]\n"
    "v_mfma_f32_16x16x32_bf16 v[160:163], v[36:39], v[156:159], v[160:163]\n"
    "v_mfma_f32_16x16x32_bf16 v[164:167], v[44:47], v[156:159], v[164:167]\n"
    "s_nop 3\n"
    "ds_read_b128 v[152:155], v14\n"
    "ds_read_b128 v[156:159], v14 offset:128\n"
    "s_waitcnt lgkmcnt(2)\n"
    "v_mfma_f32_16x16x32_bf16 v[160:163], v[48:51], v[144:147], v[160:163]\n"
    "v_mfma_f32_16x16x32_bf16 v[164:167], v[56:59], v[144:147], v[164:167]\n"
    "v_mfma_f32_16x16x32_bf16 v[160:163], v[52:55], v[148:151], v[160:163]\n"
    "v_mfma_f32_16x16x32_bf16 v[164:167], v[60:63], v[148:151], v[164:167]\n"
    "s_waitcnt lgkmcnt(0)\n"
    "v_mfma_f32_16x16x32_bf16 v[160:163], v[64:67], v[152:155], v[160:163]\n"
    "v_mfma_f32_16x16x32_bf16 v[164:167], v[72:75], v[152:155], v[164:167]\n"
    "v_mfma_f32_16x16x32_bf16 v[160:163], v[68:71], v[156:159], v[160:163]\n"
    "v_mfma_f32_16x16x32_bf16 v[164:167], v[76:79], v[156:159], v[164:167]\n"
    "buffer_load_dwordx4 v[128:131], v6, s[16:19], s67 offen nt\n"
    "s_nop 6\n"
    "v_permlane32_swap_b32 v160, v164\n"
    "v_permlane32_swap_b32 v161, v165\n"
    "v_permlane32_swap_b32 v162, v166\n"
    "v_permlane32_swap_b32 v163, v167\n"
    "v_mov_b32_dpp v160, v160 quad_perm:[0,0,2,2] row_mask:0xf bank_mask:0xf bound_ctrl:1\n"
    "v_mov_b32_dpp v161, v161 quad_perm:[0,0,2,2] row_mask:0xf bank_mask:0xf bound_ctrl:1\n"
    "v_mov_b32_dpp v162, v162 quad_perm:[0,0,2,2] row_mask:0xf bank_mask:0xf bound_ctrl:1\n"
    "v_mov_b32_dpp v163, v163 quad_perm:[0,0,2,2] row_mask:0xf bank_mask:0xf bound_ctrl:1\n"
    "v_add_f32_dpp v160, v164, v160 quad_perm:[1,1,3,3] row_mask:0xf bank_mask:0xf bound_ctrl:1\n"
    "v_add_f32_dpp v161, v165, v161 quad_perm:[1,1,3,3] row_mask:0xf bank_mask:0xf bound_ctrl:1\n"
    "v_add_f32_dpp v162, v166, v162 quad_perm:[1,1,3,3] row_mask:0xf bank_mask:0xf bound_ctrl:1\n"
    "v_add_f32_dpp v163, v167, v163 quad_perm:[1,1,3,3] row_mask:0xf bank_mask:0xf bound_ctrl:1\n"
    "v_cvt_pk_bf16_f32 v164, v160, v161\n"
    "v_lshlrev_b32 v160, 16, v164\n"
    "v_and_b32 v161, 0xffff0000, v164\n"
    "v_cvt_pk_bf16_f32 v165, v162, v163\n"
    "v_lshlrev_b32 v162, 16, v165\n"
    "v_and_b32 v163, 0xffff0000, v165\n"
    "v_mul_f32 v160, 0x3db504f3, v160\n"
    "v_mul_f32 v161, 0x3db504f3, v161\n"
    "v_mul_f32 v162, 0x3db504f3, v162\n"
    "v_mul_f32 v163, 0x3db504f3, v163\n"
    "v_cvt_pk_bf16_f32 v164, v160, v161\n"
    "v_lshlrev_b32 v160, 16, v164\n"
    "v_and_b32 v161, 0xffff0000, v164\n"
    "v_cvt_pk_bf16_f32 v165, v162, v163\n"
    "v_lshlrev_b32 v162, 16, v165\n"
    "v_and_b32 v163, 0xffff0000, v165\n"
    "v_max3_f32 v164, v160, v161, v162\n"
    "v_max_f32 v164, v164, v163\n"
    "s_nop 1\n"
    "v_max_f32_dpp v164, v164, v164 quad_perm:[2,3,0,1] row_mask:0xf bank_mask:0xf bound_ctrl:1\n"
    "s_nop 1\n"
    "v_max_f32_dpp v164, v164, v164 row_half_mirror row_mask:0xf bank_mask:0xf bound_ctrl:1\n"
    "v_mov_b32 v165, v164\n"
    "s_nop 1\n"
    "v_permlane16_swap_b32 v164, v165\n"
    "v_max_f32 v164, v164, v165\n"
    "v_mov_b32 v165, v164\n"
    "s_nop 1\n"
    "v_permlane32_swap_b32 v164, v165\n"
    "v_max_f32 v164, v164, v165\n"
    "v_sub_f32 v160, v160, v164\n"
    "v_sub_f32 v161, v161, v164\n"
    "v_sub_f32 v162, v162, v164\n"
    "v_sub_f32 v163, v163, v164\n"
    "v_mul_f32 v160, 0x3fb8aa3b, v160\n"
    "v_mul_f32 v161, 0x3fb8aa3b, v161\n"
    "v_mul_f32 v162, 0x3fb8aa3b, v162\n"
    "v_mul_f32 v163, 0x3fb8aa3b, v163\n"
    "v_exp_f32 v160, v160\n"
    "v_exp_f32 v161, v161\n"
    "v_exp_f32 v162, v162\n"
    "v_exp_f32 v163, v163\n"
    "s_waitcnt vmcnt(3)\n"
    "buffer_load_dwordx4 v[132:135], v6, s[16:19], s67 offen offset:1024 nt\n"
    "v_add_f32 v165, v160, v161\n"
    "v_add_f32 v166, v162, v163\n"
    "v_cvt_pk_bf16_f32 v152, v160, v160\n"
    "v_cvt_pk_bf16_f32 v156, v160, v160\n"
    "v_cvt_pk_bf16_f32 v153, v161, v161\n"
    "v_cvt_pk_bf16_f32 v157, v161, v161\n"
    "v_cvt_pk_bf16_f32 v154, v162, v162\n"
    "v_cvt_pk_bf16_f32 v158, v162, v162\n"
    "v_cvt_pk_bf16_f32 v155, v163, v163\n"
    "v_cvt_pk_bf16_f32 v159, v163, v163\n"
    "v_add_f32 v165, v165, v166\n"
    "v_mov_b32_dpp v152, v152 row_shr:4 row_mask:0xf bank_mask:0xa\n"
    "v_mov_b32_dpp v156, v156 row_shl:4 row_mask:0xf bank_mask:0x5\n"
    "v_mov_b32_dpp v153, v153 row_shr:4 row_mask:0xf bank_mask:0xa\n"
    "v_mov_b32_dpp v157, v157 row_shl:4 row_mask:0xf bank_mask:0x5\n"
    "v_mov_b32_dpp v154, v154 row_shr:4 row_mask:0xf bank_mask:0xa\n"
    "v_mov_b32_dpp v158, v158 row_shl:4 row_mask:0xf bank_mask:0x5\n"
    "v_mov_b32_dpp v155, v155 row_shr:4 row_mask:0xf bank_mask:0xa\n"
    "v_mov_b32_dpp v159, v159 row_shl:4 row_mask:0xf bank_mask:0x5\n"
    "v_add_f32_dpp v165, v165, v165 quad_perm:[2,3,0,1] row_mask:0xf bank_mask:0xf bound_ctrl:1\n"
    "s_nop 1\n"
    "v_add_f32_dpp v165, v165, v165 row_half_mirror row_mask:0xf bank_mask:0xf bound_ctrl:1\n"
    "v_mov_b32 v166, v165\n"
    "s_nop 1\n"
    "v_permlane16_swap_b32 v165, v166\n"
    "v_add_f32 v165, v165, v166\n"
    "v_mov_b32 v166, v165\n"
    "s_nop 1\n"
    "v_permlane32_swap_b32 v165, v166\n"
    "v_add_f32 v165, v165, v166\n"
    "s_lshl_b32 s57, s48, 2\n"
    "v_lshl_add_u32 v12, v1, 2, s57\n"
    "v_mov_b32 v13, 0\n"
    "v_mov_b32 v14, 1\n"
    "s_mov_b64 exec, 0x101\n"
    "ds_write2_b32 v12, v164, v165 offset0:128 offset1:144\n"
    "s_mov_b64 exec, 1\n"
    "ds_add_u32 v13, v14 offset:644\n"
    "s_mov_b64 exec, -1\n"
    "v_mov_b32 v160, 0\n"
    "v_mov_b32 v161, 0\n"
    "v_mov_b32 v162, 0\n"
    "v_mov_b32 v163, 0\n"
    "s_lshl_b32 s57, s48, 10\n"
    "v_add_u32 v164, s57, v6\n"
    "s_movk_i32 s93, 0x3ff\n"
    "s_and_b32 s92, s54, s93\n"
    "v_mov_b32 v0, s92\n"
    "s_cmp_eq_u32 s53, 1\n"
    "s_cbranch_scc1 .LA1_110_%=\n"
    "s_lshl_b32 s57, s48, 13\n"
    "s_add_u32 s57, s57, 23040\n"
    "v_add_u32 v48, s57, v6\n"
    "ds_read_b128 v[16:19], v48 offset:0\n"
    "ds_read_b128 v[20:23], v48 offset:1024\n"
    "ds_read_b128 v[24:27], v48 offset:2048\n"
    "ds_read_b128 v[28:31], v48 offset:3072\n"
    "ds_read_b128 v[32:35], v48 offset:4096\n"
    "ds_read_b128 v[36:39], v48 offset:5120\n"
    "ds_read_b128 v[40:43], v48 offset:6144\n"
    "ds_read_b128 v[44:47], v48 offset:7168\n"
    "s_lshr_b32 s74, s48, 1\n"
    "s_lshl2_add_u32 s74, s49, s74\n"
    "s_lshr_b32 s57, s48, 1\n"
    "s_lshl_b32 s57, s57, 6\n"
    "v_and_b32 v55, 48, v1\n"
    "v_add_u32 v55, s57, v55\n"
    "v_mov_b32 v54, 0\n"
    "v_and_b32 v12, 15, v1\n"
    "s_lshl_b32 s74, s74, 13\n"
    "s_and_b32 s57, s48, 1\n"
    "s_lshl_b32 s57, s57, 8\n"
    "s_add_u32 s74, s74, s57\n"
    "s_add_u32 s74, s74, 73728\n"
    "s_add_u32 s75, s74, 0x1000\n"
    "v_lshrrev_b32 v49, 4, v1\n"
    "v_lshlrev_b32 v49, 9, v49\n"
    "v_lshl_add_u32 v49, v12, 4, v49\n"
    "s_waitcnt vmcnt(3)\n"
    "buffer_load_dwordx4 v[136:139], v6, s[16:19], s67 offen offset:2048 nt\n"
    "v_and_b32_dpp v144, v152, v2 quad_perm:[0,0,0,0] row_mask:0xf bank_mask:0xf bound_ctrl:1\n"
    "v_and_b32_dpp v145, v152, v3 quad_perm:[0,0,0,0] row_mask:0xf bank_mask:0xf bound_ctrl:1\n"
    "v_and_b32_dpp v146, v153, v2 quad_perm:[0,0,0,0] row_mask:0xf bank_mask:0xf bound_ctrl:1\n"
    "v_and_b32_dpp v147, v153, v3 quad_perm:[0,0,0,0] row_mask:0xf bank_mask:0xf bound_ctrl:1\n"
    "v_and_b32_dpp v148, v154, v2 quad_perm:[0,0,0,0] row_mask:0xf bank_mask:0xf bound_ctrl:1\n"
    "v_and_b32_dpp v149, v154, v3 quad_perm:[0,0,0,0] row_mask:0xf bank_mask:0xf bound_ctrl:1\n"
    "v_and_b32_dpp v150, v155, v2 quad_perm:[0,0,0,0] row_mask:0xf bank_mask:0xf bound_ctrl:1\n"
    "v_and_b32_dpp v151, v155, v3 quad_perm:[0,0,0,0] row_mask:0xf bank_mask:0xf bound_ctrl:1\n"
    "s_waitcnt vmcnt(13)\n"
    "v_smfmac_f32_16x16x64_bf16 v[160:163], v[144:147], v[80:87], v4\n"
    "v_and_b32_dpp v144, v152, v2 quad_perm:[2,2,2,2] row_mask:0xf bank_mask:0xf bound_ctrl:1\n"
    "v_and_b32_dpp v145, v152, v3 quad_perm:[2,2,2,2] row_mask:0xf bank_mask:0xf bound_ctrl:1\n"
    "v_and_b32_dpp v146, v153, v2 quad_perm:[2,2,2,2] row_mask:0xf bank_mask:0xf bound_ctrl:1\n"
    "v_and_b32_dpp v147, v153, v3 quad_perm:[2,2,2,2] row_mask:0xf bank_mask:0xf bound_ctrl:1\n"
    "s_waitcnt vmcnt(11)\n"
    "v_smfmac_f32_16x16x64_bf16 v[160:163], v[148:151], v[88:95], v4\n"
    "v_and_b32_dpp v148, v154, v2 quad_perm:[2,2,2,2] row_mask:0xf bank_mask:0xf bound_ctrl:1\n"
    "v_and_b32_dpp v149, v154, v3 quad_perm:[2,2,2,2] row_mask:0xf bank_mask:0xf bound_ctrl:1\n"
    "v_and_b32_dpp v150, v155, v2 quad_perm:[2,2,2,2] row_mask:0xf bank_mask:0xf bound_ctrl:1\n"
    "v_and_b32_dpp v151, v155, v3 quad_perm:[2,2,2,2] row_mask:0xf bank_mask:0xf bound_ctrl:1\n"
    "s_waitcnt vmcnt(9)\n"
    "v_smfmac_f32_16x16x64_bf16 v[160:163], v[144:147], v[96:103], v4\n"
    "s_waitcnt vmcnt(3)\n"
    "buffer_load_dwordx4 v[140:143], v6, s[16:19], s67 offen offset:3072 nt\n"
    "v_and_b32_dpp v144, v156, v2 quad_perm:[0,0,0,0] row_mask:0xf bank_mask:0xf bound_ctrl:1\n"
    "v_and_b32_dpp v145, v156, v3 quad_perm:[0,0,0,0] row_mask:0xf bank_mask:0xf bound_ctrl:1\n"
    "v_and_b32_dpp v146, v157, v2 quad_perm:[0,0,0,0] row_mask:0xf bank_mask:0xf bound_ctrl:1\n"
    "v_and_b32_dpp v147, v157, v3 quad_perm:[0,0,0,0] row_mask:0xf bank_mask:0xf bound_ctrl:1\n"
    "s_waitcnt vmcnt(8)\n"
    "v_smfmac_f32_16x16x64_bf16 v[160:163], v[148:151], v[104:111], v4\n"
    "v_and_b32_dpp v148, v158, v2 quad_perm:[0,0,0,0] row_mask:0xf bank_mask:0xf bound_ctrl:1\n"
    "v_and_b32_dpp v149, v158, v3 quad_perm:[0,0,0,0] row_mask:0xf bank_mask:0xf bound_ctrl:1\n"
    "v_and_b32_dpp v150, v159, v2 quad_perm:[0,0,0,0] row_mask:0xf bank_mask:0xf bound_ctrl:1\n"
    "v_and_b32_dpp v151, v159, v3 quad_perm:[0,0,0,0] row_mask:0xf bank_mask:0xf bound_ctrl:1\n"
    "s_waitcnt vmcnt(6)\n"
    "v_smfmac_f32_16x16x64_bf16 v[160:163], v[144:147], v[112:119], v4\n"
    "v_and_b32_dpp v144, v156, v2 quad_perm:[2,2,2,2] row_mask:0xf bank_mask:0xf bound_ctrl:1\n"
    "v_and_b32_dpp v145, v156, v3 quad_perm:[2,2,2,2] row_mask:0xf bank_mask:0xf bound_ctrl:1\n"
    "v_and_b32_dpp v146, v157, v2 quad_perm:[2,2,2,2] row_mask:0xf bank_mask:0xf bound_ctrl:1\n"
    "v_and_b32_dpp v147, v157, v3 quad_perm:[2,2,2,2] row_mask:0xf bank_mask:0xf bound_ctrl:1\n"
    "s_waitcnt vmcnt(4)\n"
    "v_smfmac_f32_16x16x64_bf16 v[160:163], v[148:151], v[120:127], v4\n"
    "v_and_b32_dpp v148, v158, v2 quad_perm:[2,2,2,2] row_mask:0xf bank_mask:0xf bound_ctrl:1\n"
    "v_and_b32_dpp v149, v158, v3 quad_perm:[2,2,2,2] row_mask:0xf bank_mask:0xf bound_ctrl:1\n"
    "v_and_b32_dpp v150, v159, v2 quad_perm:[2,2,2,2] row_mask:0xf bank_mask:0xf bound_ctrl:1\n"
    "v_and_b32_dpp v151, v159, v3 quad_perm:[2,2,2,2] row_mask:0xf bank_mask:0xf bound_ctrl:1\n"
    "s_waitcnt vmcnt(2)\n"
    "v_smfmac_f32_16x16x64_bf16 v[160:163], v[144:147], v[128:135], v4\n"
    "s_waitcnt vmcnt(0)\n"
    "v_smfmac_f32_16x16x64_bf16 v[160:163], v[148:151], v[136:143], v4\n"
    "s_nop 7\n"
    "ds_write_b128 v164, v[160:163] offset:1280\n"
    "s_waitcnt lgkmcnt(0)\n"
    "s_barrier\n"
    "s_cmp_lg_u32 s53, 0\n"
    "s_cbranch_scc1 .LA2_109_%=\n"
    "s_sleep 5\n"
    ".Lts_111_%=:\n"
    "s_memrealtime s[90:91]\n"
    "s_waitcnt lgkmcnt(0)\n"
    "s_sub_u32 s90, s90, s63\n"
    "s_cmp_lt_u32 s90, 1290\n"
    "s_cbranch_scc1 .Lts_111_%=\n"
    "v_mov_b32 v160, 0\n"
    "v_mov_b32 v161, 0\n"
    "v_mov_b32 v162, 0\n"
    "v_mov_b32 v163, 0\n"
    "s_mov_b32 s60, 65536\n"
    "buffer_load_dwordx4 v[56:59], v49, s[20:23], s74 offen sc1\n"
    "buffer_load_dwordx4 v[60:63], v49, s[20:23], s74 offen offset:2048 sc1\n"
    "buffer_load_dwordx4 v[64:67], v49, s[20:23], s75 offen sc1\n"
    "buffer_load_dwordx4 v[68:71], v49, s[20:23], s75 offen offset:2048 sc1\n"
    "s_waitcnt lgkmcnt(0)\n"
    "ds_read_b32 v84, v54 offset:664\n"
    "ds_read_b128 v[80:83], v55 offset:17664\n"
    "s_waitcnt vmcnt(3)\n"
    "v_xor_b32 v56, s92, v56\n"
    "v_xor_b32 v57, s92, v57\n"
    "v_xor_b32 v58, s92, v58\n"
    "v_xor_b32 v59, s92, v59\n"
    "v_or3_b32 v12, v56, v57, v58\n"
    "v_bitop3_b32 v12, v12, v59, s93 bitop3:0xa8\n"
    "v_cmp_ne_u32 s[76:77], 0, v12\n"
    "s_waitcnt vmcnt(2)\n"
    "v_xor_b32 v60, s92, v60\n"
    "v_xor_b32 v61, s92, v61\n"
    "v_xor_b32 v62, s92, v62\n"
    "v_xor_b32 v63, s92, v63\n"
    "v_or3_b32 v12, v60, v61, v62\n"
    "v_bitop3_b32 v12, v12, v63, s93 bitop3:0xa8\n"
    "v_cmp_ne_u32 s[78:79], 0, v12\n"
    "s_waitcnt vmcnt(1)\n"
    "v_xor_b32 v64, s92, v64\n"
    "v_xor_b32 v65, s92, v65\n"
    "v_xor_b32 v66, s92, v66\n"
    "v_xor_b32 v67, s92, v67\n"
    "v_or3_b32 v12, v64, v65, v66\n"
    "v_bitop3_b32 v12, v12, v67, s93 bitop3:0xa8\n"
    "v_cmp_ne_u32 s[80:81], 0, v12\n"
    "s_waitcnt vmcnt(0)\n"
    "v_xor_b32 v68, s92, v68\n"
    "v_xor_b32 v69, s92, v69\n"
    "v_xor_b32 v70, s92, v70\n"
    "v_xor_b32 v71, s92, v71\n"
    "v_or3_b32 v12, v68, v69, v70\n"
    "v_bitop3_b32 v12, v12, v71, s93 bitop3:0xa8\n"
    "v_cmp_ne_u32 s[82:83], 0, v12\n"
    "s_or_b64 s[86:87], s[76:77], s[78:79]\n"
    "s_or_b64 s[86:87], s[86:87], s[80:81]\n"
    "s_or_b64 s[86:87], s[86:87], s[82:83]\n"
    "s_cbranch_scc0 .Lgd_114_%=\n"
    ".Lgp_113_%=:\n"
    "s_mov_b64 exec, s[76:77]\n"
    "s_cbranch_execz .Lgs_115_%=\n"
    "buffer_load_dwordx4 v[56:59], v49, s[20:23], s74 offen sc1\n"
    ".Lgs_115_%=:\n"
    "s_mov_b64 exec, s[78:79]\n"
    "s_cbranch_execz .Lgs_116_%=\n"
    "buffer_load_dwordx4 v[60:63], v49, s[20:23], s74 offen offset:2048 sc1\n"
    ".Lgs_116_%=:\n"
    "s_mov_b64 exec, s[80:81]\n"
    "s_cbranch_execz .Lgs_117_%=\n"
    "buffer_load_dwordx4 v[64:67], v49, s[20:23], s75 offen sc1\n"
    ".Lgs_117_%=:\n"
    "s_mov_b64 exec, s[82:83]\n"
    "s_cbranch_execz .Lgs_118_%=\n"
    "buffer_load_dwordx4 v[68:71], v49, s[20:23], s75 offen offset:2048 sc1\n"
    ".Lgs_118_%=:\n"
    "s_mov_b64 exec, -1\n"
    "s_waitcnt lgkmcnt(0)\n"
    "ds_read_b32 v84, v54 offset:664\n"
    "ds_read_b128 v[80:83], v55 offset:17664\n"
    "s_waitcnt vmcnt(0)\n"
    "s_mov_b64 exec, s[76:77]\n"
    "s_cbranch_execz .Lgs_119_%=\n"
    "v_xor_b32 v56, s92, v56\n"
    "v_xor_b32 v57, s92, v57\n"
    "v_xor_b32 v58, s92, v58\n"
    "v_xor_b32 v59, s92, v59\n"
    "v_or3_b32 v12, v56, v57, v58\n"
    "v_bitop3_b32 v12, v12, v59, s93 bitop3:0xa8\n"
    "v_cmp_ne_u32 s[76:77], 0, v12\n"
    ".Lgs_119_%=:\n"
    "s_mov_b64 exec, s[78:79]\n"
    "s_cbranch_execz .Lgs_120_%=\n"
    "v_xor_b32 v60, s92, v60\n"
    "v_xor_b32 v61, s92, v61\n"
    "v_xor_b32 v62, s92, v62\n"
    "v_xor_b32 v63, s92, v63\n"
    "v_or3_b32 v12, v60, v61, v62\n"
    "v_bitop3_b32 v12, v12, v63, s93 bitop3:0xa8\n"
    "v_cmp_ne_u32 s[78:79], 0, v12\n"
    ".Lgs_120_%=:\n"
    "s_mov_b64 exec, s[80:81]\n"
    "s_cbranch_execz .Lgs_121_%=\n"
    "v_xor_b32 v64, s92, v64\n"
    "v_xor_b32 v65, s92, v65\n"
    "v_xor_b32 v66, s92, v66\n"
    "v_xor_b32 v67, s92, v67\n"
    "v_or3_b32 v12, v64, v65, v66\n"
    "v_bitop3_b32 v12, v12, v67, s93 bitop3:0xa8\n"
    "v_cmp_ne_u32 s[80:81], 0, v12\n"
    ".Lgs_121_%=:\n"
    "s_mov_b64 exec, s[82:83]\n"
    "s_cbranch_execz .Lgs_122_%=\n"
    "v_xor_b32 v68, s92, v68\n"
    "v_xor_b32 v69, s92, v69\n"
    "v_xor_b32 v70, s92, v70\n"
    "v_xor_b32 v71, s92, v71\n"
    "v_or3_b32 v12, v68, v69, v70\n"
    "v_bitop3_b32 v12, v12, v71, s93 bitop3:0xa8\n"
    "v_cmp_ne_u32 s[82:83], 0, v12\n"
    ".Lgs_122_%=:\n"
    "s_nop 0\n"
    "s_or_b64 s[86:87], s[76:77], s[78:79]\n"
    "s_or_b64 s[86:87], s[86:87], s[80:81]\n"
    "s_or_b64 s[86:87], s[86:87], s[82:83]\n"
    "s_cbranch_scc0 .Lgd_114_%=\n"
    "s_sub_u32 s60, s60, 1\n"
    "s_cbranch_scc0 .Lgp_113_%=\n"
    ".Lgd_114_%=:\n"
    "s_mov_b64 exec, -1\n"
    "s_waitcnt lgkmcnt(0)\n"
    "v_cmp_ne_u32 vcc, 0, v84\n"
    "s_cbranch_vccnz .Lwok_123_%=\n"
    "s_mov_b32 s60, 4194304\n"
    ".Lwf_124_%=:\n"
    "ds_read_b32 v13, v54 offset:664\n"
    "ds_read_b128 v[80:83], v55 offset:17664\n"
    "s_waitcnt lgkmcnt(0)\n"
    "v_cmp_ne_u32 vcc, 0, v13\n"
    "s_cbranch_vccnz .Lwfd_125_%=\n"
    "s_sub_u32 s60, s60, 1\n"
    "s_cbranch_scc0 .Lwf_124_%=\n"
    ".Lwfd_125_%=:\n"
    ".Lwok_123_%=:\n"
    "v_pk_mul_f32 v[72:73], v[56:57], v[80:81] op_sel_hi:[1,0]\n"
    "v_pk_mul_f32 v[74:75], v[58:59], v[80:81] op_sel_hi:[1,0]\n"
    "v_pk_fma_f32 v[72:73], v[60:61], v[80:81], v[72:73] op_sel:[0,1,0] op_sel_hi:[1,1,1]\n"
    "v_pk_fma_f32 v[74:75], v[62:63], v[80:81], v[74:75] op_sel:[0,1,0] op_sel_hi:[1,1,1]\n"
    "v_pk_mul_f32 v[76:77], v[64:65], v[82:83] op_sel_hi:[1,0]\n"
    "v_pk_mul_f32 v[78:79], v[66:67], v[82:83] op_sel_hi:[1,0]\n"
    "v_pk_fma_f32 v[76:77], v[68:69], v[82:83], v[76:77] op_sel:[0,1,0] op_sel_hi:[1,1,1]\n"
    "v_pk_fma_f32 v[78:79], v[70:71], v[82:83], v[78:79] op_sel:[0,1,0] op_sel_hi:[1,1,1]\n"
    "v_pk_add_f32 v[72:73], v[72:73], v[76:77]\n"
    "v_pk_add_f32 v[74:75], v[74:75], v[78:79]\n"
    "s_nop 1\n"
    "v_permlane32_swap_b32 v72, v74\n"
    "v_permlane32_swap_b32 v73, v75\n"
    "v_add_f32 v72, v72, v74\n"
    "v_add_f32 v73, v73, v75\n"
    "s_nop 1\n"
    "v_permlane16_swap_b32 v72, v73\n"
    "v_add_f32 v72, v72, v73\n"
    "v_cvt_pk_bf16_f32 v73, v72, v72\n"
    "v_cvt_pk_bf16_f32 v74, v72, v72\n"
    "s_nop 0\n"
    "v_mov_b32_dpp v73, v73 row_shr:4 row_mask:0xf bank_mask:0xa\n"
    "v_mov_b32_dpp v74, v74 row_shl:4 row_mask:0xf bank_mask:0x5\n"
    "s_nop 0\n"
    "v_and_b32_dpp v144, v73, v2 quad_perm:[0,0,0,0] row_mask:0xf bank_mask:0xf bound_ctrl:1\n"
    "v_and_b32_dpp v145, v73, v3 quad_perm:[0,0,0,0] row_mask:0xf bank_mask:0xf bound_ctrl:1\n"
    "v_and_b32_dpp v146, v73, v2 quad_perm:[1,1,1,1] row_mask:0xf bank_mask:0xf bound_ctrl:1\n"
    "v_and_b32_dpp v147, v73, v3 quad_perm:[1,1,1,1] row_mask:0xf bank_mask:0xf bound_ctrl:1\n"
    "v_and_b32_dpp v148, v73, v2 quad_perm:[2,2,2,2] row_mask:0xf bank_mask:0xf bound_ctrl:1\n"
    "v_and_b32_dpp v149, v73, v3 quad_perm:[2,2,2,2] row_mask:0xf bank_mask:0xf bound_ctrl:1\n"
    "v_and_b32_dpp v150, v73, v2 quad_perm:[3,3,3,3] row_mask:0xf bank_mask:0xf bound_ctrl:1\n"
    "v_and_b32_dpp v151, v73, v3 quad_perm:[3,3,3,3] row_mask:0xf bank_mask:0xf bound_ctrl:1\n"
    "v_smfmac_f32_16x16x64_bf16 v[160:163], v[144:147], v[16:23], v4\n"
    "v_and_b32_dpp v152, v74, v2 quad_perm:[0,0,0,0] row_mask:0xf bank_mask:0xf bound_ctrl:1\n"
    "v_and_b32_dpp v153, v74, v3 quad_perm:[0,0,0,0] row_mask:0xf bank_mask:0xf bound_ctrl:1\n"
    "v_and_b32_dpp v154, v74, v2 quad_perm:[1,1,1,1] row_mask:0xf bank_mask:0xf bound_ctrl:1\n"
    "v_and_b32_dpp v155, v74, v3 quad_perm:[1,1,1,1] row_mask:0xf bank_mask:0xf bound_ctrl:1\n"
    "v_smfmac_f32_16x16x64_bf16 v[160:163], v[148:151], v[24:31], v4\n"
    "v_and_b32_dpp v156, v74, v2 quad_perm:[2,2,2,2] row_mask:0xf bank_mask:0xf bound_ctrl:1\n"
    "v_and_b32_dpp v157, v74, v3 quad_perm:[2,2,2,2] row_mask:0xf bank_mask:0xf bound_ctrl:1\n"
    "v_and_b32_dpp v158, v74, v2 quad_perm:[3,3,3,3] row_mask:0xf bank_mask:0xf bound_ctrl:1\n"
    "v_and_b32_dpp v159, v74, v3 quad_perm:[3,3,3,3] row_mask:0xf bank_mask:0xf bound_ctrl:1\n"
    "v_smfmac_f32_16x16x64_bf16 v[160:163], v[152:155], v[32:39], v4\n"
    "s_nop 0\n"
    "v_smfmac_f32_16x16x64_bf16 v[160:163], v[156:159], v[40:47], v4\n"
    "s_mov_b64 exec, s[58:59]\n"
    "s_nop 6\n"
    "ds_write_b128 v7, v[160:163] offset:9472\n"
    "s_mov_b64 exec, -1\n"
    "s_waitcnt lgkmcnt(0)\n"
    "s_barrier\n"
    "s_endpgm\n"
    ".LA2_109_%=:\n"
    "s_endpgm\n"
    ".LA1_110_%=:\n"
    "v_lshlrev_b32 v165, 4, v8\n"
    "v_lshl_add_u32 v165, v9, 3, v165\n"
    "v_add_u32 v165, v165, v10\n"
    "v_mul_u32_u24 v165, 1152, v165\n"
    "v_lshl_add_u32 v165, v11, 4, v165\n"
    "s_mul_i32 s70, s56, 73728\n"
    "s_lshl_b32 s57, s48, 7\n"
    "s_add_u32 s70, s70, s57\n"
    "s_waitcnt vmcnt(3)\n"
    "buffer_load_dwordx4 v[136:139], v6, s[16:19], s67 offen offset:2048 nt\n"
    "v_and_b32_dpp v144, v152, v2 quad_perm:[0,0,0,0] row_mask:0xf bank_mask:0xf bound_ctrl:1\n"
    "v_and_b32_dpp v145, v152, v3 quad_perm:[0,0,0,0] row_mask:0xf bank_mask:0xf bound_ctrl:1\n"
    "v_and_b32_dpp v146, v153, v2 quad_perm:[0,0,0,0] row_mask:0xf bank_mask:0xf bound_ctrl:1\n"
    "v_and_b32_dpp v147, v153, v3 quad_perm:[0,0,0,0] row_mask:0xf bank_mask:0xf bound_ctrl:1\n"
    "v_and_b32_dpp v148, v154, v2 quad_perm:[0,0,0,0] row_mask:0xf bank_mask:0xf bound_ctrl:1\n"
    "v_and_b32_dpp v149, v154, v3 quad_perm:[0,0,0,0] row_mask:0xf bank_mask:0xf bound_ctrl:1\n"
    "v_and_b32_dpp v150, v155, v2 quad_perm:[0,0,0,0] row_mask:0xf bank_mask:0xf bound_ctrl:1\n"
    "v_and_b32_dpp v151, v155, v3 quad_perm:[0,0,0,0] row_mask:0xf bank_mask:0xf bound_ctrl:1\n"
    "s_waitcnt vmcnt(13)\n"
    "v_smfmac_f32_16x16x64_bf16 v[160:163], v[144:147], v[80:87], v4\n"
    "v_and_b32_dpp v144, v152, v2 quad_perm:[2,2,2,2] row_mask:0xf bank_mask:0xf bound_ctrl:1\n"
    "v_and_b32_dpp v145, v152, v3 quad_perm:[2,2,2,2] row_mask:0xf bank_mask:0xf bound_ctrl:1\n"
    "v_and_b32_dpp v146, v153, v2 quad_perm:[2,2,2,2] row_mask:0xf bank_mask:0xf bound_ctrl:1\n"
    "v_and_b32_dpp v147, v153, v3 quad_perm:[2,2,2,2] row_mask:0xf bank_mask:0xf bound_ctrl:1\n"
    "s_waitcnt vmcnt(11)\n"
    "v_smfmac_f32_16x16x64_bf16 v[160:163], v[148:151], v[88:95], v4\n"
    "v_and_b32_dpp v148, v154, v2 quad_perm:[2,2,2,2] row_mask:0xf bank_mask:0xf bound_ctrl:1\n"
    "v_and_b32_dpp v149, v154, v3 quad_perm:[2,2,2,2] row_mask:0xf bank_mask:0xf bound_ctrl:1\n"
    "v_and_b32_dpp v150, v155, v2 quad_perm:[2,2,2,2] row_mask:0xf bank_mask:0xf bound_ctrl:1\n"
    "v_and_b32_dpp v151, v155, v3 quad_perm:[2,2,2,2] row_mask:0xf bank_mask:0xf bound_ctrl:1\n"
    "s_waitcnt vmcnt(9)\n"
    "v_smfmac_f32_16x16x64_bf16 v[160:163], v[144:147], v[96:103], v4\n"
    "s_waitcnt vmcnt(3)\n"
    "buffer_load_dwordx4 v[140:143], v6, s[16:19], s67 offen offset:3072 nt\n"
    "v_and_b32_dpp v144, v156, v2 quad_perm:[0,0,0,0] row_mask:0xf bank_mask:0xf bound_ctrl:1\n"
    "v_and_b32_dpp v145, v156, v3 quad_perm:[0,0,0,0] row_mask:0xf bank_mask:0xf bound_ctrl:1\n"
    "v_and_b32_dpp v146, v157, v2 quad_perm:[0,0,0,0] row_mask:0xf bank_mask:0xf bound_ctrl:1\n"
    "v_and_b32_dpp v147, v157, v3 quad_perm:[0,0,0,0] row_mask:0xf bank_mask:0xf bound_ctrl:1\n"
    "s_waitcnt vmcnt(8)\n"
    "v_smfmac_f32_16x16x64_bf16 v[160:163], v[148:151], v[104:111], v4\n"
    "v_and_b32_dpp v148, v158, v2 quad_perm:[0,0,0,0] row_mask:0xf bank_mask:0xf bound_ctrl:1\n"
    "v_and_b32_dpp v149, v158, v3 quad_perm:[0,0,0,0] row_mask:0xf bank_mask:0xf bound_ctrl:1\n"
    "v_and_b32_dpp v150, v159, v2 quad_perm:[0,0,0,0] row_mask:0xf bank_mask:0xf bound_ctrl:1\n"
    "v_and_b32_dpp v151, v159, v3 quad_perm:[0,0,0,0] row_mask:0xf bank_mask:0xf bound_ctrl:1\n"
    "s_waitcnt vmcnt(6)\n"
    "v_smfmac_f32_16x16x64_bf16 v[160:163], v[144:147], v[112:119], v4\n"
    "v_and_b32_dpp v144, v156, v2 quad_perm:[2,2,2,2] row_mask:0xf bank_mask:0xf bound_ctrl:1\n"
    "v_and_b32_dpp v145, v156, v3 quad_perm:[2,2,2,2] row_mask:0xf bank_mask:0xf bound_ctrl:1\n"
    "v_and_b32_dpp v146, v157, v2 quad_perm:[2,2,2,2] row_mask:0xf bank_mask:0xf bound_ctrl:1\n"
    "v_and_b32_dpp v147, v157, v3 quad_perm:[2,2,2,2] row_mask:0xf bank_mask:0xf bound_ctrl:1\n"
    "s_waitcnt vmcnt(4)\n"
    "v_smfmac_f32_16x16x64_bf16 v[160:163], v[148:151], v[120:127], v4\n"
    "v_and_b32_dpp v148, v158, v2 quad_perm:[2,2,2,2] row_mask:0xf bank_mask:0xf bound_ctrl:1\n"
    "v_and_b32_dpp v149, v158, v3 quad_perm:[2,2,2,2] row_mask:0xf bank_mask:0xf bound_ctrl:1\n"
    "v_and_b32_dpp v150, v159, v2 quad_perm:[2,2,2,2] row_mask:0xf bank_mask:0xf bound_ctrl:1\n"
    "v_and_b32_dpp v151, v159, v3 quad_perm:[2,2,2,2] row_mask:0xf bank_mask:0xf bound_ctrl:1\n"
    "s_waitcnt vmcnt(2)\n"
    "v_smfmac_f32_16x16x64_bf16 v[160:163], v[144:147], v[128:135], v4\n"
    "s_waitcnt vmcnt(0)\n"
    "v_smfmac_f32_16x16x64_bf16 v[160:163], v[148:151], v[136:143], v4\n"
    "s_nop 7\n"
    "ds_write_b128 v164, v[160:163] offset:1280\n"
    "s_waitcnt lgkmcnt(0)\n"
    "s_barrier\n"
    "s_sleep 4\n"
    ".LM2_126_%=:\n"
    "buffer_load_dwordx4 v[48:51], v165, s[44:47], s70 offen nt\n"
    "s_add_u32 s57, s70, 36864\n"
    "buffer_load_dwordx4 v[52:55], v165, s[44:47], s57 offen nt\n"
    "buffer_load_dwordx4 v[56:59], v165, s[44:47], s70 offen offset:2304 nt\n"
    "s_add_u32 s57, s70, 39168\n"
    "buffer_load_dwordx4 v[60:63], v165, s[44:47], s57 offen nt\n"
    "s_add_u32 s57, s70, 4608\n"
    "buffer_load_dwordx4 v[64:67], v165, s[44:47], s57 offen nt\n"
    "s_add_u32 s57, s70, 41472\n"
    "buffer_load_dwordx4 v[68:71], v165, s[44:47], s57 offen nt\n"
    "s_add_u32 s57, s70, 6912\n"
    "buffer_load_dwordx4 v[72:75], v165, s[44:47], s57 offen nt\n"
    "s_add_u32 s57, s70, 43776\n"
    "buffer_load_dwordx4 v[76:79], v165, s[44:47], s57 offen nt\n"
    "v_and_b32 v12, 15, v1\n"
    "v_lshrrev_b32 v14, 4, v1\n"
    "v_lshlrev_b32 v124, 10, v10\n"
    "v_lshl_add_u32 v124, v8, 7, v124\n"
    "v_lshl_add_u32 v124, v12, 3, v124\n"
    "s_lshl_b32 s57, s48, 13\n"
    "s_add_u32 s57, s57, 23040\n"
    "v_add_u32 v124, s57, v124\n"
    "v_add_u32 v125, 256, v124\n"
    "ds_read2st64_b64 v[16:19], v124 offset0:0 offset1:1\n"
    "ds_read2st64_b64 v[20:23], v125 offset0:0 offset1:1\n"
    "ds_read2st64_b64 v[24:27], v124 offset0:4 offset1:5\n"
    "ds_read2st64_b64 v[28:31], v125 offset0:4 offset1:5\n"
    "ds_read2st64_b64 v[32:35], v124 offset0:8 offset1:9\n"
    "ds_read2st64_b64 v[36:39], v125 offset0:8 offset1:9\n"
    "ds_read2st64_b64 v[40:43], v124 offset0:12 offset1:13\n"
    "ds_read2st64_b64 v[44:47], v125 offset0:12 offset1:13\n"
    "v_lshl_add_u32 v121, v12, 2, v14\n"
    "s_lshl_b32 s57, s48, 6\n"
    "v_add_u32 v121, s57, v121\n"
    "v_lshlrev_b32 v121, 1, v121\n"
    "v_lshlrev_b32 v127, 2, v1\n"
    "ds_read_u16 v112, v121 offset:14080\n"
    "ds_read_u16 v114, v121 offset:15232\n"
    "ds_read_u16 v120, v121 offset:16384\n"
    "ds_read2st64_b32 v[128:129], v127 offset0:69 offset1:70\n"
    "v_lshlrev_b32 v91, 4, v12\n"
    "s_lshl_b32 s71, s48, 8\n"
    "v_mul_u32_u24 v123, 2304, v14\n"
    "v_add_u32 v123, v123, v91\n"
    "v_add_u32 v126, s71, v91\n"
    "s_waitcnt lgkmcnt(0)\n"
    "v_lshlrev_b32 v112, 16, v112\n"
    "v_lshlrev_b32 v114, 16, v114\n"
    "v_lshlrev_b32 v120, 16, v120\n"
    "v_add_f32 v114, 1.0, v114\n"
    "v_cvt_pk_bf16_f32 v114, v114, v114\n"
    "v_lshlrev_b32 v114, 16, v114\n"
    "v_mov_b32 v160, 0\n"
    "v_mov_b32 v161, 0\n"
    "v_mov_b32 v162, 0\n"
    "v_mov_b32 v163, 0\n"
    "v_mov_b32 v164, 0\n"
    "v_mov_b32 v165, 0\n"
    "v_mov_b32 v166, 0\n"
    "v_mov_b32 v167, 0\n"
    "v_lshlrev_b32 v93, 5, v11\n"
    "v_lshl_add_u32 v93, v10, 4, v93\n"
    "v_lshl_add_u32 v93, v8, 3, v93\n"
    "s_mov_b32 s88, 0x00ff00ff\n"
    "s_mov_b32 s89, 0x00ff00ff\n"
    "v_lshrrev_b32 v90, 1, v91\n"
    "s_lshl_b32 s57, s48, 3\n"
    "v_lshl_add_u32 v92, v10, 2, s57\n"
    "s_mul_i32 s72, s56, 2304\n"
    "s_add_u32 s72, s72, s71\n"
    "s_add_u32 s72, s72, 387072\n"
    "s_and_b32 s57, s49, 3\n"
    "s_mul_i32 s57, s57, 18432\n"
    "s_add_u32 s74, s71, s57\n"
    "s_add_u32 s74, s74, 552960\n"
    "s_add_u32 s75, s74, 9216\n"
    ".Lts_127_%=:\n"
    "s_memrealtime s[90:91]\n"
    "s_waitcnt lgkmcnt(0)\n"
    "s_sub_u32 s90, s90, s63\n"
    "s_cmp_lt_u32 s90, 1397\n"
    "s_cbranch_scc1 .Lts_127_%=\n"
    "s_mov_b32 s60, 65536\n"
    "buffer_load_dwordx4 v[96:99], v123, s[20:23], s74 offen sc1\n"
    "buffer_load_dwordx4 v[100:103], v123, s[20:23], s75 offen sc1\n"
    "s_waitcnt vmcnt(1)\n"
    "v_xor_b32 v96, s92, v96\n"
    "v_xor_b32 v97, s92, v97\n"
    "v_xor_b32 v98, s92, v98\n"
    "v_xor_b32 v99, s92, v99\n"
    "v_or3_b32 v12, v96, v97, v98\n"
    "v_bitop3_b32 v12, v12, v99, s93 bitop3:0xa8\n"
    "v_cmp_ne_u32 s[76:77], 0, v12\n"
    "s_waitcnt vmcnt(0)\n"
    "v_xor_b32 v100, s92, v100\n"
    "v_xor_b32 v101, s92, v101\n"
    "v_xor_b32 v102, s92, v102\n"
    "v_xor_b32 v103, s92, v103\n"
    "v_or3_b32 v12, v100, v101, v102\n"
    "v_bitop3_b32 v12, v12, v103, s93 bitop3:0xa8\n"
    "v_cmp_ne_u32 s[78:79], 0, v12\n"
    "s_nop 0\n"
    "s_or_b64 s[86:87], s[76:77], s[78:79]\n"
    "s_cbranch_scc0 .Lgd_130_%=\n"
    ".Lgp_129_%=:\n"
    "s_mov_b64 exec, s[76:77]\n"
    "s_cbranch_execz .Lgs_131_%=\n"
    "buffer_load_dwordx4 v[96:99], v123, s[20:23], s74 offen sc1\n"
    ".Lgs_131_%=:\n"
    "s_mov_b64 exec, s[78:79]\n"
    "s_cbranch_execz .Lgs_132_%=\n"
    "buffer_load_dwordx4 v[100:103], v123, s[20:23], s75 offen sc1\n"
    ".Lgs_132_%=:\n"
    "s_waitcnt vmcnt(0)\n"
    "s_mov_b64 exec, s[76:77]\n"
    "s_cbranch_execz .Lgs_133_%=\n"
    "v_xor_b32 v96, s92, v96\n"
    "v_xor_b32 v97, s92, v97\n"
    "v_xor_b32 v98, s92, v98\n"
    "v_xor_b32 v99, s92, v99\n"
    "v_or3_b32 v12, v96, v97, v98\n"
    "v_bitop3_b32 v12, v12, v99, s93 bitop3:0xa8\n"
    "v_cmp_ne_u32 s[76:77], 0, v12\n"
    ".Lgs_133_%=:\n"
    "s_mov_b64 exec, s[78:79]\n"
    "s_cbranch_execz .Lgs_134_%=\n"
    "v_xor_b32 v100, s92, v100\n"
    "v_xor_b32 v101, s92, v101\n"
    "v_xor_b32 v102, s92, v102\n"
    "v_xor_b32 v103, s92, v103\n"
    "v_or3_b32 v12, v100, v101, v102\n"
    "v_bitop3_b32 v12, v12, v103, s93 bitop3:0xa8\n"
    "v_cmp_ne_u32 s[78:79], 0, v12\n"
    ".Lgs_134_%=:\n"
    "s_nop 0\n"
    "s_or_b64 s[86:87], s[76:77], s[78:79]\n"
    "s_cbranch_scc0 .Lgd_130_%=\n"
    "s_sub_u32 s60, s60, 1\n"
    "s_cbranch_scc0 .Lgp_129_%=\n"
    ".Lgd_130_%=:\n"
    "s_mov_b64 exec, -1\n"
    "v_pk_add_f32 v[104:105], v[96:97], v[100:101]\n"
    "v_pk_add_f32 v[106:107], v[98:99], v[102:103]\n"
    "s_nop 1\n"
    "v_permlane32_swap_b32 v104, v106\n"
    "v_permlane32_swap_b32 v105, v107\n"
    "v_add_f32 v104, v104, v106\n"
    "v_add_f32 v105, v105, v107\n"
    "s_nop 1\n"
    "v_permlane16_swap_b32 v104, v105\n"
    "v_add_f32 v104, v104, v105\n"
    "v_fma_f32 v104, v112, v104, v120\n"
    "v_mul_f32 v106, v104, v114\n"
    "v_mul_f32 v105, v104, v104\n"
    "v_cvt_pk_bf16_f32 v107, v106, 0\n"
    "v_add_f32_dpp v104, v104, v104 quad_perm:[1,0,3,2] row_mask:0xf bank_mask:0xf bound_ctrl:1\n"
    "v_add_f32_dpp v105, v105, v105 quad_perm:[1,0,3,2] row_mask:0xf bank_mask:0xf bound_ctrl:1\n"
    "v_mov_b32_dpp v144, v107 row_newbcast:0 row_mask:0xf bank_mask:0xf\n"
    "v_mov_b32_dpp v145, v107 row_newbcast:1 row_mask:0xf bank_mask:0xf\n"
    "v_mov_b32_dpp v146, v107 row_newbcast:2 row_mask:0xf bank_mask:0xf\n"
    "v_mov_b32_dpp v147, v107 row_newbcast:3 row_mask:0xf bank_mask:0xf\n"
    "v_mov_b32_dpp v148, v107 row_newbcast:4 row_mask:0xf bank_mask:0xf\n"
    "v_mov_b32_dpp v149, v107 row_newbcast:5 row_mask:0xf bank_mask:0xf\n"
    "v_mov_b32_dpp v150, v107 row_newbcast:6 row_mask:0xf bank_mask:0xf\n"
    "v_mov_b32_dpp v151, v107 row_newbcast:7 row_mask:0xf bank_mask:0xf\n"
    "v_smfmac_f32_16x16x64_bf16 v[160:163], v[144:147], v[16:23], v4\n"
    "v_mov_b32_dpp v152, v107 row_newbcast:8 row_mask:0xf bank_mask:0xf\n"
    "v_mov_b32_dpp v153, v107 row_newbcast:9 row_mask:0xf bank_mask:0xf\n"
    "v_mov_b32_dpp v154, v107 row_newbcast:10 row_mask:0xf bank_mask:0xf\n"
    "v_mov_b32_dpp v155, v107 row_newbcast:11 row_mask:0xf bank_mask:0xf\n"
    "v_smfmac_f32_16x16x64_bf16 v[160:163], v[148:151], v[24:31], v4\n"
    "v_mov_b32_dpp v156, v107 row_newbcast:12 row_mask:0xf bank_mask:0xf\n"
    "v_mov_b32_dpp v157, v107 row_newbcast:13 row_mask:0xf bank_mask:0xf\n"
    "v_mov_b32_dpp v158, v107 row_newbcast:14 row_mask:0xf bank_mask:0xf\n"
    "v_mov_b32_dpp v159, v107 row_newbcast:15 row_mask:0xf bank_mask:0xf\n"
    "v_smfmac_f32_16x16x64_bf16 v[160:163], v[152:155], v[32:39], v4\n"
    "v_add_f32_dpp v104, v104, v104 quad_perm:[2,3,0,1] row_mask:0xf bank_mask:0xf bound_ctrl:1\n"
    "v_smfmac_f32_16x16x64_bf16 v[160:163], v[156:159], v[40:47], v4\n"
    "v_add_f32_dpp v105, v105, v105 quad_perm:[2,3,0,1] row_mask:0xf bank_mask:0xf bound_ctrl:1\n"
    "v_add_f32_dpp v104, v104, v104 row_half_mirror row_mask:0xf bank_mask:0xf bound_ctrl:1\n"
    "s_nop 0\n"
    "v_add_f32_dpp v105, v105, v105 row_half_mirror row_mask:0xf bank_mask:0xf bound_ctrl:1\n"
    "v_add_f32_dpp v104, v104, v104 row_ror:8 row_mask:0xf bank_mask:0xf bound_ctrl:1\n"
    "s_nop 0\n"
    "v_add_f32_dpp v105, v105, v105 row_ror:8 row_mask:0xf bank_mask:0xf bound_ctrl:1\n"
    "s_mov_b64 exec, 0xffff\n"
    "ds_write_b128 v126, v[160:163] offset:9472\n"
    "s_mov_b64 exec, -1\n"
    "v_permlane16_swap_b32 v104, v105\n"
    "v_add_f32 v104, v104, v105\n"
    "v_mov_b32 v105, v104\n"
    "s_nop 1\n"
    "v_permlane32_swap_b32 v104, v105\n"
    "v_add_f32 v104, v104, v105\n"
    "s_mov_b64 exec, 0x10001\n"
    "ds_write_b32 v92, v104 offset:768\n"
    "s_mov_b64 exec, -1\n"
    "s_waitcnt lgkmcnt(0)\n"
    "s_barrier\n"
    "ds_read_b64 v[110:111], v90 offset:768\n"
    "ds_read2st64_b32 v[96:97], v127 offset0:37 offset1:38\n"
    "ds_read2st64_b32 v[98:99], v127 offset0:39 offset1:40\n"
    "ds_read2st64_b32 v[100:101], v127 offset0:41 offset1:42\n"
    "ds_read2st64_b32 v[102:103], v127 offset0:43 offset1:44\n"
    "ds_read_b32 v104, v127 offset:11520\n"
    "s_waitcnt lgkmcnt(5)\n"
    "v_add_f32_dpp v110, v110, v110 quad_perm:[1,0,3,2] row_mask:0xf bank_mask:0xf bound_ctrl:1\n"
    "v_add_f32_dpp v111, v111, v111 quad_perm:[1,0,3,2] row_mask:0xf bank_mask:0xf bound_ctrl:1\n"
    "s_nop 0\n"
    "v_add_f32_dpp v110, v110, v110 quad_perm:[2,3,0,1] row_mask:0xf bank_mask:0xf bound_ctrl:1\n"
    "v_add_f32_dpp v111, v111, v111 quad_perm:[2,3,0,1] row_mask:0xf bank_mask:0xf bound_ctrl:1\n"
    "s_nop 0\n"
    "v_add_f32_dpp v110, v110, v110 row_half_mirror row_mask:0xf bank_mask:0xf bound_ctrl:1\n"
    "v_add_f32_dpp v111, v111, v111 row_half_mirror row_mask:0xf bank_mask:0xf bound_ctrl:1\n"
    "s_nop 0\n"
    "v_add_f32_dpp v110, v110, v110 row_ror:8 row_mask:0xf bank_mask:0xf bound_ctrl:1\n"
    "v_add_f32_dpp v111, v111, v111 row_ror:8 row_mask:0xf bank_mask:0xf bound_ctrl:1\n"
    "v_mul_f32 v110, 0x3ae38e39, v110\n"
    "v_mul_f32 v111, 0x3ae38e39, v111\n"
    "v_fma_f32 v111, -v110, v110, v111\n"
    "v_max_f32 v111, 0, v111\n"
    "v_cvt_pk_bf16_f32 v111, v111, v110\n"
    "s_waitcnt lgkmcnt(0)\n"
    "v_pk_add_f32 v[96:97], v[96:97], v[98:99]\n"
    "v_and_b32 v110, 0xffff0000, v111\n"
    "v_lshlrev_b32 v111, 16, v111\n"
    "v_pk_add_f32 v[100:101], v[100:101], v[102:103]\n"
    "v_add_f32 v111, 0x3727c5ac, v111\n"
    "v_cvt_pk_bf16_f32 v111, v111, v111\n"
    "v_pk_add_f32 v[96:97], v[96:97], v[100:101]\n"
    "v_lshlrev_b32 v111, 16, v111\n"
    "v_sqrt_f32 v111, v111\n"
    "v_add_f32 v96, v96, v104\n"
    "v_cvt_pk_bf16_f32 v111, v111, v111\n"
    "v_add_f32 v96, v96, v97\n"
    "v_lshlrev_b32 v111, 16, v111\n"
    "v_rcp_f32 v111, v111\n"
    "v_fma_f32 v96, -v110, v128, v96\n"
    "v_fma_f32 v96, v111, v96, v129\n"
    "v_cvt_pk_bf16_f32 v96, v96, v96\n"
    "v_lshlrev_b32 v96, 16, v96\n"
    "v_mul_f32 v97, 0xbfb8aa3b, v96\n"
    "v_exp_f32 v97, v97\n"
    "s_nop 0\n"
    "v_add_f32 v97, 1.0, v97\n"
    "v_rcp_f32 v97, v97\n"
    "s_nop 0\n"
    "v_mul_f32 v96, v96, v97\n"
    "v_cvt_pk_bf16_f32 v97, v96, v96\n"
    "v_cvt_pk_bf16_f32 v96, v96, v96\n"
    "s_nop 1\n"
    "v_mov_b32_dpp v96, v96 row_shr:4 row_mask:0xf bank_mask:0xa\n"
    "v_mov_b32_dpp v97, v97 row_shl:4 row_mask:0xf bank_mask:0x5\n"
    "s_nop 0\n"
    "v_and_b32_dpp v144, v96, v2 quad_perm:[0,0,0,0] row_mask:0xf bank_mask:0xf bound_ctrl:1\n"
    "v_and_b32_dpp v145, v96, v3 quad_perm:[0,0,0,0] row_mask:0xf bank_mask:0xf bound_ctrl:1\n"
    "v_and_b32_dpp v146, v96, v2 quad_perm:[1,1,1,1] row_mask:0xf bank_mask:0xf bound_ctrl:1\n"
    "v_and_b32_dpp v147, v96, v3 quad_perm:[1,1,1,1] row_mask:0xf bank_mask:0xf bound_ctrl:1\n"
    "v_and_b32_dpp v148, v96, v2 quad_perm:[2,2,2,2] row_mask:0xf bank_mask:0xf bound_ctrl:1\n"
    "v_and_b32_dpp v149, v96, v3 quad_perm:[2,2,2,2] row_mask:0xf bank_mask:0xf bound_ctrl:1\n"
    "v_and_b32_dpp v150, v96, v2 quad_perm:[3,3,3,3] row_mask:0xf bank_mask:0xf bound_ctrl:1\n"
    "v_and_b32_dpp v151, v96, v3 quad_perm:[3,3,3,3] row_mask:0xf bank_mask:0xf bound_ctrl:1\n"
    "v_smfmac_f32_16x16x64_bf16 v[164:167], v[144:147], v[48:55], v4\n"
    "v_and_b32_dpp v152, v97, v2 quad_perm:[0,0,0,0] row_mask:0xf bank_mask:0xf bound_ctrl:1\n"
    "v_and_b32_dpp v153, v97, v3 quad_perm:[0,0,0,0] row_mask:0xf bank_mask:0xf bound_ctrl:1\n"
    "v_and_b32_dpp v154, v97, v2 quad_perm:[1,1,1,1] row_mask:0xf bank_mask:0xf bound_ctrl:1\n"
    "v_and_b32_dpp v155, v97, v3 quad_perm:[1,1,1,1] row_mask:0xf bank_mask:0xf bound_ctrl:1\n"
    "v_smfmac_f32_16x16x64_bf16 v[164:167], v[148:151], v[56:63], v4\n"
    "v_and_b32_dpp v156, v97, v2 quad_perm:[2,2,2,2] row_mask:0xf bank_mask:0xf bound_ctrl:1\n"
    "v_and_b32_dpp v157, v97, v3 quad_perm:[2,2,2,2] row_mask:0xf bank_mask:0xf bound_ctrl:1\n"
    "v_and_b32_dpp v158, v97, v2 quad_perm:[3,3,3,3] row_mask:0xf bank_mask:0xf bound_ctrl:1\n"
    "v_and_b32_dpp v159, v97, v3 quad_perm:[3,3,3,3] row_mask:0xf bank_mask:0xf bound_ctrl:1\n"
    "v_smfmac_f32_16x16x64_bf16 v[164:167], v[152:155], v[64:71], v4\n"
    "s_nop 0\n"
    "v_smfmac_f32_16x16x64_bf16 v[164:167], v[156:159], v[72:79], v4\n"
    "s_nop 7\n"
    "v_mov_b32_dpp v164, v164 row_ror:8 row_mask:0xc bank_mask:0xf\n"
    "v_mov_b32_dpp v165, v165 row_ror:8 row_mask:0xc bank_mask:0xf\n"
    "v_mov_b32_dpp v166, v166 row_ror:8 row_mask:0xc bank_mask:0xf\n"
    "v_mov_b32_dpp v167, v167 row_ror:8 row_mask:0xc bank_mask:0xf\n"
    "s_nop 0\n"
    "v_permlane32_swap_b32 v164, v166\n"
    "v_permlane32_swap_b32 v165, v167\n"
    "s_mov_b64 exec, s[88:89]\n"
    "v_add_f32 v164, v164, v166\n"
    "v_add_f32 v165, v165, v167\n"
    "v_bfi_b32 v164, s93, v0, v164\n"
    "v_bfi_b32 v165, s93, v0, v165\n"
    "buffer_store_dwordx2 v[164:165], v93, s[20:23], s72 offen sc1\n"
    "s_mov_b64 exec, -1\n"
    "s_endpgm\n"
    ".LS_1_%=:\n"
    "s_setprio 3\n"
    "buffer_load_dword v124, off, s[20:23], 0 sc1\n"
    "v_bfe_u32 v13, v1, 2, 1\n"
    "v_mul_u32_u24 v3, 0xffff, v13\n"
    "v_xor_b32 v2, 0xffff, v3\n"
    "v_and_b32 v13, 3, v1\n"
    "v_mul_u32_u24 v4, 0x5555, v13\n"
    "s_mov_b32 s58, 0x00ff00ff\n"
    "s_mov_b32 s59, 0xff00ff00\n"
    "v_lshlrev_b32 v7, 8, v8\n"
    "v_lshl_add_u32 v7, v11, 5, v7\n"
    "v_lshl_add_u32 v7, v10, 4, v7\n"
    "s_lshl_b32 s57, s48, 9\n"
    "v_add_u32 v7, s57, v7\n"
    "v_lshlrev_b32 v127, 2, v1\n"
    "s_cmp_eq_u32 s53, 2\n"
    "s_cbranch_scc1 .Ls2_135_%=\n"
    "s_cmp_eq_u32 s53, 3\n"
    "s_cbranch_scc1 .Ls3_136_%=\n"
    "s_cmp_lg_u32 s53, 0\n"
    "s_cbranch_scc1 .Lsn_139_%=\n"
    "s_waitcnt vmcnt(3)\n"
    "v_lshlrev_b32 v161, 16, v160\n"
    "v_and_b32 v160, 0xffff0000, v160\n"
    "v_mul_f32 v162, 0xbfb8aa3b, v161\n"
    "v_exp_f32 v162, v162\n"
    "s_nop 0\n"
    "v_add_f32 v162, 1.0, v162\n"
    "v_min_f32 v162, 0x7f000000, v162\n"
    "v_rcp_f32 v163, v162\n"
    "s_nop 0\n"
    "v_mul_f32 v13, v161, v163\n"
    "v_fma_f32 v161, -v13, v162, v161\n"
    "v_fma_f32 v161, v161, v163, v13\n"
    "v_mul_f32 v162, 0xbfb8aa3b, v160\n"
    "v_exp_f32 v162, v162\n"
    "s_nop 0\n"
    "v_add_f32 v162, 1.0, v162\n"
    "v_min_f32 v162, 0x7f000000, v162\n"
    "v_rcp_f32 v163, v162\n"
    "s_nop 0\n"
    "v_mul_f32 v13, v160, v163\n"
    "v_fma_f32 v160, -v13, v162, v160\n"
    "v_fma_f32 v160, v160, v163, v13\n"
    "v_cvt_pk_bf16_f32 v161, v161, v161\n"
    "v_cvt_pk_bf16_f32 v160, v160, v160\n"
    "v_mov_b32 v152, 0\n"
    "v_mov_b32 v153, 0\n"
    "v_mov_b32 v154, 0\n"
    "v_mov_b32 v155, 0\n"
    "v_and_b32 v156, v161, v2\n"
    "v_and_b32 v157, v161, v3\n"
    "v_and_b32 v158, v160, v2\n"
    "v_and_b32 v159, v160, v3\n"
    "v_mov_b32 v12, 0\n"
    "v_mov_b32 v13, 1\n"
    "s_waitcnt vmcnt(1)\n"
    "v_smfmac_f32_16x16x64_bf16 v[152:155], v[156:159], v[136:143], v4\n"
    "s_mov_b64 exec, s[58:59]\n"
    "s_nop 6\n"
    "ds_write_b128 v7, v[152:155] offset:9472\n"
    "s_mov_b64 exec, 1\n"
    "ds_add_u32 v12, v13 offset:648\n"
    "s_mov_b64 exec, -1\n"
    "s_mov_b32 s60, 4194304\n"
    "v_mov_b32 v12, 0\n"
    ".Llw_141_%=:\n"
    "ds_read_b32 v13, v12 offset:692\n"
    "s_waitcnt lgkmcnt(0)\n"
    "v_cmp_ne_u32 vcc, 0, v13\n"
    "s_cbranch_vccnz .Llwd_142_%=\n"
    "s_sleep 2\n"
    "s_sub_u32 s60, s60, 1\n"
    "s_cbranch_scc0 .Llw_141_%=\n"
    ".Llwd_142_%=:\n"
    "v_readfirstlane_b32 s62, v13\n"
    "s_waitcnt vmcnt(0) lgkmcnt(0)\n"
    "v_readfirstlane_b32 s54, v124\n"
    "s_nop 0\n"
    "s_add_u32 s54, s54, 1\n"
    "s_cmp_eq_u32 s54, 0\n"
    "s_cselect_b32 s54, 1, s54\n"
    "s_mov_b32 s60, 4194304\n"
    "v_mov_b32 v12, 0\n"
    ".Llw_143_%=:\n"
    "ds_read_b32 v13, v12 offset:648\n"
    "s_waitcnt lgkmcnt(0)\n"
    "v_cmp_eq_u32 vcc, 9, v13\n"
    "s_cbranch_vccnz .Llwd_144_%=\n"
    "s_sleep 2\n"
    "s_sub_u32 s60, s60, 1\n"
    "s_cbranch_scc0 .Llw_143_%=\n"
    ".Llwd_144_%=:\n"
    "ds_read2st64_b32 v[88:89], v127 offset0:37 offset1:38\n"
    "ds_read2st64_b32 v[90:91], v127 offset0:39 offset1:40\n"
    "ds_read2st64_b32 v[92:93], v127 offset0:41 offset1:42\n"
    "ds_read2st64_b32 v[94:95], v127 offset0:43 offset1:44\n"
    "ds_read2st64_b32 v[96:97], v127 offset0:45 offset1:46\n"
    "ds_read2st64_b32 v[98:99], v127 offset0:47 offset1:48\n"
    "ds_read2st64_b32 v[100:101], v127 offset0:49 offset1:50\n"
    "ds_read2st64_b32 v[102:103], v127 offset0:51 offset1:52\n"
    "ds_read2st64_b32 v[104:105], v127 offset0:53 offset1:54\n"
    "s_and_b32 s57, s2, 3\n"
    "s_mul_i32 s57, s57, 9216\n"
    "s_lshr_b32 s61, s2, 2\n"
    "s_lshl_b32 s61, s61, 9\n"
    "s_add_u32 s57, s57, s61\n"
    "s_add_u32 s72, s57, 8192\n"
    "s_waitcnt lgkmcnt(0)\n"
    "v_pk_add_f32 v[88:89], v[88:89], v[90:91]\n"
    "v_pk_add_f32 v[92:93], v[92:93], v[94:95]\n"
    "v_pk_add_f32 v[96:97], v[96:97], v[98:99]\n"
    "v_pk_add_f32 v[100:101], v[100:101], v[102:103]\n"
    "v_pk_add_f32 v[88:89], v[88:89], v[92:93]\n"
    "v_pk_add_f32 v[96:97], v[96:97], v[100:101]\n"
    "v_pk_add_f32 v[88:89], v[88:89], v[96:97]\n"
    "v_pk_add_f32 v[88:89], v[88:89], v[104:105]\n"
    "v_add_f32 v88, v88, v89\n"
    "v_mov_b32 v89, s54\n"
    "buffer_store_dwordx2 v[88:89], v125, s[20:23], s72 offen sc1\n"
    "s_branch .Lsq_140_%=\n"
    ".Ls2_135_%=:\n"
    "s_mov_b32 s76, 45056\n"
    "s_lshl_b32 s77, s56, 8\n"
    "s_add_u32 s77, s77, 57344\n"
    "v_and_b32 v117, 15, v1\n"
    "v_lshlrev_b32 v117, 4, v117\n"
    "v_lshrrev_b32 v126, 2, v1\n"
    "v_lshlrev_b32 v126, 4, v126\n"
    "v_bfe_u32 v12, v1, 1, 1\n"
    "v_lshl_add_u32 v126, v12, 3, v126\n"
    "v_and_b32 v12, 1, v1\n"
    "v_add_u32 v126, v126, v12\n"
    "v_lshlrev_b32 v126, 2, v126\n"
    "v_add_u32 v126, 18176, v126\n"
    "v_add_u32 v118, 1024, v126\n"
    "v_add_u32 v119, 2048, v126\n"
    "s_mov_b32 s60, 4194304\n"
    "v_mov_b32 v12, 0\n"
    ".Llw_145_%=:\n"
    "ds_read_b32 v13, v12 offset:692\n"
    "s_waitcnt lgkmcnt(0)\n"
    "v_cmp_ne_u32 vcc, 0, v13\n"
    "s_cbranch_vccnz .Llwd_146_%=\n"
    "s_sleep 2\n"
    "s_sub_u32 s60, s60, 1\n"
    "s_cbranch_scc0 .Llw_145_%=\n"
    ".Llwd_146_%=:\n"
    "v_readfirstlane_b32 s62, v13\n"
    "s_waitcnt vmcnt(0) lgkmcnt(0)\n"
    "v_readfirstlane_b32 s54, v124\n"
    "s_nop 0\n"
    "s_add_u32 s54, s54, 1\n"
    "s_cmp_eq_u32 s54, 0\n"
    "s_cselect_b32 s54, 1, s54\n"
    ".Ltw_147_%=:\n"
    "s_memrealtime s[90:91]\n"
    "s_waitcnt lgkmcnt(0)\n"
    "s_sub_u32 s90, s90, s62\n"
    "s_cmp_ge_u32 s90, 410\n"
    "s_cbranch_scc1 .Ltwd_148_%=\n"
    "s_sleep 1\n"
    "s_branch .Ltw_147_%=\n"
    ".Ltwd_148_%=:\n"
    "s_mov_b32 s60, 65536\n"
    ".Ltp_149_%=:\n"
    "buffer_load_dwordx4 v[128:131], v6, s[20:23], s76 offen sc1\n"
    "buffer_load_dwordx4 v[132:135], v6, s[20:23], s76 offen offset:1024 sc1\n"
    "buffer_load_dwordx4 v[136:139], v117, s[20:23], s76 offen offset:2048 sc1\n"
    "s_waitcnt vmcnt(0)\n"
    "v_xor_b32 v12, s54, v129\n"
    "v_xor_b32 v13, s54, v131\n"
    "v_or_b32 v12, v12, v13\n"
    "v_xor_b32 v13, s54, v133\n"
    "v_or_b32 v12, v12, v13\n"
    "v_xor_b32 v13, s54, v135\n"
    "v_or_b32 v12, v12, v13\n"
    "v_xor_b32 v13, s54, v137\n"
    "v_or_b32 v12, v12, v13\n"
    "v_xor_b32 v13, s54, v139\n"
    "v_or_b32 v12, v12, v13\n"
    "v_cmp_ne_u32 vcc, 0, v12\n"
    "s_cbranch_vccz .Ltpd_150_%=\n"
    "s_sleep 12\n"
    "s_sub_u32 s60, s60, 1\n"
    "s_cbranch_scc0 .Ltp_149_%=\n"
    ".Ltpd_150_%=:\n"
    "v_and_b32 v100, 0xffff, v128\n"
    "v_lshrrev_b32 v101, 16, v128\n"
    "v_and_b32 v102, 0xffff, v130\n"
    "v_lshrrev_b32 v103, 16, v130\n"
    "v_and_b32 v104, 0xffff, v132\n"
    "v_lshrrev_b32 v105, 16, v132\n"
    "v_and_b32 v106, 0xffff, v134\n"
    "v_lshrrev_b32 v107, 16, v134\n"
    "v_and_b32 v108, 0xffff, v136\n"
    "v_lshrrev_b32 v109, 16, v136\n"
    "v_and_b32 v110, 0xffff, v138\n"
    "v_lshrrev_b32 v111, 16, v138\n"
    "s_branch .Lsg_138_%=\n"
    ".Ls4_137_%=:\n"
    "v_lshrrev_b32 v126, 2, v1\n"
    "v_lshlrev_b32 v126, 4, v126\n"
    "v_bfe_u32 v12, v1, 1, 1\n"
    "v_lshl_add_u32 v126, v12, 3, v126\n"
    "v_and_b32 v12, 1, v1\n"
    "v_add_u32 v126, v126, v12\n"
    "v_lshlrev_b32 v126, 2, v126\n"
    "v_add_u32 v126, 18176, v126\n"
    "v_add_u32 v118, 1024, v126\n"
    "v_add_u32 v119, 2048, v126\n"
    "v_add_u32 v115, 14080, v125\n"
    "v_mov_b32 v17, v18\n"
    "v_mov_b32 v41, v42\n"
    "ds_write_b64 v115, v[16:17] offset:0\n"
    "ds_write_b64 v115, v[40:41] offset:1152\n"
    "ds_write_b64 v115, v[148:149] offset:2304\n"
    "v_mov_b32 v21, v22\n"
    "v_mov_b32 v45, v46\n"
    "ds_write_b64 v115, v[20:21] offset:512\n"
    "ds_write_b64 v115, v[44:45] offset:1664\n"
    "ds_write_b64 v115, v[150:151] offset:2816\n"
    "s_mov_b64 exec, 0xffff\n"
    "v_mov_b32 v25, v26\n"
    "v_mov_b32 v49, v50\n"
    "ds_write_b64 v115, v[24:25] offset:1024\n"
    "ds_write_b64 v115, v[48:49] offset:2176\n"
    "ds_write_b64 v115, v[152:153] offset:3328\n"
    "s_mov_b64 exec, -1\n"
    "v_and_b32 v100, 0xffff, v28\n"
    "v_lshrrev_b32 v101, 16, v28\n"
    "v_and_b32 v102, 0xffff, v30\n"
    "v_lshrrev_b32 v103, 16, v30\n"
    "v_and_b32 v104, 0xffff, v32\n"
    "v_lshrrev_b32 v105, 16, v32\n"
    "v_and_b32 v106, 0xffff, v34\n"
    "v_lshrrev_b32 v107, 16, v34\n"
    "v_and_b32 v108, 0xffff, v36\n"
    "v_lshrrev_b32 v109, 16, v36\n"
    "v_and_b32 v110, 0xffff, v38\n"
    "v_lshrrev_b32 v111, 16, v38\n"
    "v_add_u32 v126, 2432, v126\n"
    "v_add_u32 v118, 2432, v118\n"
    "v_add_u32 v119, 2432, v119\n"
    "ds_write2_b32 v126, v100, v101 offset0:0 offset1:2\n"
    "ds_write2_b32 v126, v102, v103 offset0:4 offset1:6\n"
    "ds_write2_b32 v118, v104, v105 offset0:0 offset1:2\n"
    "ds_write2_b32 v118, v106, v107 offset0:4 offset1:6\n"
    "s_mov_b64 exec, 0xffff\n"
    "ds_write2_b32 v119, v108, v109 offset0:0 offset1:2\n"
    "ds_write2_b32 v119, v110, v111 offset0:4 offset1:6\n"
    "s_mov_b64 exec, -1\n"
    "v_subrev_u32 v126, 2432, v126\n"
    "v_subrev_u32 v118, 2432, v118\n"
    "v_subrev_u32 v119, 2432, v119\n"
    "v_lshlrev_b32 v100, 16, v40\n"
    "v_and_b32 v101, 0xffff0000, v40\n"
    "v_lshlrev_b32 v102, 16, v42\n"
    "v_and_b32 v103, 0xffff0000, v42\n"
    "v_lshlrev_b32 v104, 16, v44\n"
    "v_and_b32 v105, 0xffff0000, v44\n"
    "v_lshlrev_b32 v106, 16, v46\n"
    "v_and_b32 v107, 0xffff0000, v46\n"
    "v_lshlrev_b32 v108, 16, v48\n"
    "v_and_b32 v109, 0xffff0000, v48\n"
    "v_lshlrev_b32 v110, 16, v50\n"
    "v_and_b32 v111, 0xffff0000, v50\n"
    "v_add_f32 v100, 1.0, v100\n"
    "v_cvt_pk_bf16_f32 v100, v100, 0\n"
    "v_add_f32 v101, 1.0, v101\n"
    "v_cvt_pk_bf16_f32 v101, v101, 0\n"
    "v_add_f32 v102, 1.0, v102\n"
    "v_cvt_pk_bf16_f32 v102, v102, 0\n"
    "v_add_f32 v103, 1.0, v103\n"
    "v_cvt_pk_bf16_f32 v103, v103, 0\n"
    "v_add_f32 v104, 1.0, v104\n"
    "v_cvt_pk_bf16_f32 v104, v104, 0\n"
    "v_add_f32 v105, 1.0, v105\n"
    "v_cvt_pk_bf16_f32 v105, v105, 0\n"
    "v_add_f32 v106, 1.0, v106\n"
    "v_cvt_pk_bf16_f32 v106, v106, 0\n"
    "v_add_f32 v107, 1.0, v107\n"
    "v_cvt_pk_bf16_f32 v107, v107, 0\n"
    "v_add_f32 v108, 1.0, v108\n"
    "v_cvt_pk_bf16_f32 v108, v108, 0\n"
    "v_add_f32 v109, 1.0, v109\n"
    "v_cvt_pk_bf16_f32 v109, v109, 0\n"
    "v_add_f32 v110, 1.0, v110\n"
    "v_cvt_pk_bf16_f32 v110, v110, 0\n"
    "v_add_f32 v111, 1.0, v111\n"
    "v_cvt_pk_bf16_f32 v111, v111, 0\n"
    "s_branch .Lsg_138_%=\n"
    ".Ls3_136_%=:\n"
    "buffer_load_dwordx2 v[88:89], v125, s[8:11], 0 offen\n"
    "buffer_load_dwordx2 v[90:91], v125, s[8:11], 0 offen offset:512\n"
    "buffer_load_dwordx2 v[92:93], v125, s[8:11], 0 offen offset:1024\n"
    "s_lshl_b32 s77, s56, 8\n"
    "s_add_u32 s77, s77, 48128\n"
    "v_lshrrev_b32 v126, 2, v1\n"
    "v_lshlrev_b32 v126, 4, v126\n"
    "v_bfe_u32 v12, v1, 1, 1\n"
    "v_lshl_add_u32 v126, v12, 3, v126\n"
    "v_and_b32 v12, 1, v1\n"
    "v_add_u32 v126, v126, v12\n"
    "v_lshlrev_b32 v126, 2, v126\n"
    "v_add_u32 v126, 18176, v126\n"
    "v_add_u32 v118, 1024, v126\n"
    "v_add_u32 v119, 2048, v126\n"
    "s_mov_b32 s60, 4194304\n"
    "v_mov_b32 v12, 0\n"
    ".Llw_151_%=:\n"
    "ds_read_b32 v13, v12 offset:692\n"
    "s_waitcnt lgkmcnt(0)\n"
    "v_cmp_ne_u32 vcc, 0, v13\n"
    "s_cbranch_vccnz .Llwd_152_%=\n"
    "s_sleep 2\n"
    "s_sub_u32 s60, s60, 1\n"
    "s_cbranch_scc0 .Llw_151_%=\n"
    ".Llwd_152_%=:\n"
    "v_readfirstlane_b32 s62, v13\n"
    "s_waitcnt vmcnt(3) lgkmcnt(0)\n"
    "v_readfirstlane_b32 s54, v124\n"
    "s_nop 0\n"
    "s_add_u32 s54, s54, 1\n"
    "s_cmp_eq_u32 s54, 0\n"
    "s_cselect_b32 s54, 1, s54\n"
    "s_waitcnt vmcnt(0)\n"
    "v_lshlrev_b32 v100, 16, v88\n"
    "v_and_b32 v101, 0xffff0000, v88\n"
    "v_lshlrev_b32 v102, 16, v89\n"
    "v_and_b32 v103, 0xffff0000, v89\n"
    "v_lshlrev_b32 v104, 16, v90\n"
    "v_and_b32 v105, 0xffff0000, v90\n"
    "v_lshlrev_b32 v106, 16, v91\n"
    "v_and_b32 v107, 0xffff0000, v91\n"
    "v_lshlrev_b32 v108, 16, v92\n"
    "v_and_b32 v109, 0xffff0000, v92\n"
    "v_lshlrev_b32 v110, 16, v93\n"
    "v_and_b32 v111, 0xffff0000, v93\n"
    "v_mul_f32 v112, 0xbfb8aa3b, v100\n"
    "v_exp_f32 v112, v112\n"
    "s_nop 0\n"
    "v_add_f32 v112, 1.0, v112\n"
    "v_min_f32 v112, 0x7f000000, v112\n"
    "v_rcp_f32 v113, v112\n"
    "s_nop 0\n"
    "v_mul_f32 v13, v100, v113\n"
    "v_fma_f32 v100, -v13, v112, v100\n"
    "v_fma_f32 v100, v100, v113, v13\n"
    "v_cvt_pk_bf16_f32 v100, v100, 0\n"
    "v_mul_f32 v112, 0xbfb8aa3b, v101\n"
    "v_exp_f32 v112, v112\n"
    "s_nop 0\n"
    "v_add_f32 v112, 1.0, v112\n"
    "v_min_f32 v112, 0x7f000000, v112\n"
    "v_rcp_f32 v113, v112\n"
    "s_nop 0\n"
    "v_mul_f32 v13, v101, v113\n"
    "v_fma_f32 v101, -v13, v112, v101\n"
    "v_fma_f32 v101, v101, v113, v13\n"
    "v_cvt_pk_bf16_f32 v101, v101, 0\n"
    "v_mul_f32 v112, 0xbfb8aa3b, v102\n"
    "v_exp_f32 v112, v112\n"
    "s_nop 0\n"
    "v_add_f32 v112, 1.0, v112\n"
    "v_min_f32 v112, 0x7f000000, v112\n"
    "v_rcp_f32 v113, v112\n"
    "s_nop 0\n"
    "v_mul_f32 v13, v102, v113\n"
    "v_fma_f32 v102, -v13, v112, v102\n"
    "v_fma_f32 v102, v102, v113, v13\n"
    "v_cvt_pk_bf16_f32 v102, v102, 0\n"
    "v_mul_f32 v112, 0xbfb8aa3b, v103\n"
    "v_exp_f32 v112, v112\n"
    "s_nop 0\n"
    "v_add_f32 v112, 1.0, v112\n"
    "v_min_f32 v112, 0x7f000000, v112\n"
    "v_rcp_f32 v113, v112\n"
    "s_nop 0\n"
    "v_mul_f32 v13, v103, v113\n"
    "v_fma_f32 v103, -v13, v112, v103\n"
    "v_fma_f32 v103, v103, v113, v13\n"
    "v_cvt_pk_bf16_f32 v103, v103, 0\n"
    "v_mul_f32 v112, 0xbfb8aa3b, v104\n"
    "v_exp_f32 v112, v112\n"
    "s_nop 0\n"
    "v_add_f32 v112, 1.0, v112\n"
    "v_min_f32 v112, 0x7f000000, v112\n"
    "v_rcp_f32 v113, v112\n"
    "s_nop 0\n"
    "v_mul_f32 v13, v104, v113\n"
    "v_fma_f32 v104, -v13, v112, v104\n"
    "v_fma_f32 v104, v104, v113, v13\n"
    "v_cvt_pk_bf16_f32 v104, v104, 0\n"
    "v_mul_f32 v112, 0xbfb8aa3b, v105\n"
    "v_exp_f32 v112, v112\n"
    "s_nop 0\n"
    "v_add_f32 v112, 1.0, v112\n"
    "v_min_f32 v112, 0x7f000000, v112\n"
    "v_rcp_f32 v113, v112\n"
    "s_nop 0\n"
    "v_mul_f32 v13, v105, v113\n"
    "v_fma_f32 v105, -v13, v112, v105\n"
    "v_fma_f32 v105, v105, v113, v13\n"
    "v_cvt_pk_bf16_f32 v105, v105, 0\n"
    "v_mul_f32 v112, 0xbfb8aa3b, v106\n"
    "v_exp_f32 v112, v112\n"
    "s_nop 0\n"
    "v_add_f32 v112, 1.0, v112\n"
    "v_min_f32 v112, 0x7f000000, v112\n"
    "v_rcp_f32 v113, v112\n"
    "s_nop 0\n"
    "v_mul_f32 v13, v106, v113\n"
    "v_fma_f32 v106, -v13, v112, v106\n"
    "v_fma_f32 v106, v106, v113, v13\n"
    "v_cvt_pk_bf16_f32 v106, v106, 0\n"
    "v_mul_f32 v112, 0xbfb8aa3b, v107\n"
    "v_exp_f32 v112, v112\n"
    "s_nop 0\n"
    "v_add_f32 v112, 1.0, v112\n"
    "v_min_f32 v112, 0x7f000000, v112\n"
    "v_rcp_f32 v113, v112\n"
    "s_nop 0\n"
    "v_mul_f32 v13, v107, v113\n"
    "v_fma_f32 v107, -v13, v112, v107\n"
    "v_fma_f32 v107, v107, v113, v13\n"
    "v_cvt_pk_bf16_f32 v107, v107, 0\n"
    "v_mul_f32 v112, 0xbfb8aa3b, v108\n"
    "v_exp_f32 v112, v112\n"
    "s_nop 0\n"
    "v_add_f32 v112, 1.0, v112\n"
    "v_min_f32 v112, 0x7f000000, v112\n"
    "v_rcp_f32 v113, v112\n"
    "s_nop 0\n"
    "v_mul_f32 v13, v108, v113\n"
    "v_fma_f32 v108, -v13, v112, v108\n"
    "v_fma_f32 v108, v108, v113, v13\n"
    "v_cvt_pk_bf16_f32 v108, v108, 0\n"
    "v_mul_f32 v112, 0xbfb8aa3b, v109\n"
    "v_exp_f32 v112, v112\n"
    "s_nop 0\n"
    "v_add_f32 v112, 1.0, v112\n"
    "v_min_f32 v112, 0x7f000000, v112\n"
    "v_rcp_f32 v113, v112\n"
    "s_nop 0\n"
    "v_mul_f32 v13, v109, v113\n"
    "v_fma_f32 v109, -v13, v112, v109\n"
    "v_fma_f32 v109, v109, v113, v13\n"
    "v_cvt_pk_bf16_f32 v109, v109, 0\n"
    "v_mul_f32 v112, 0xbfb8aa3b, v110\n"
    "v_exp_f32 v112, v112\n"
    "s_nop 0\n"
    "v_add_f32 v112, 1.0, v112\n"
    "v_min_f32 v112, 0x7f000000, v112\n"
    "v_rcp_f32 v113, v112\n"
    "s_nop 0\n"
    "v_mul_f32 v13, v110, v113\n"
    "v_fma_f32 v110, -v13, v112, v110\n"
    "v_fma_f32 v110, v110, v113, v13\n"
    "v_cvt_pk_bf16_f32 v110, v110, 0\n"
    "v_mul_f32 v112, 0xbfb8aa3b, v111\n"
    "v_exp_f32 v112, v112\n"
    "s_nop 0\n"
    "v_add_f32 v112, 1.0, v112\n"
    "v_min_f32 v112, 0x7f000000, v112\n"
    "v_rcp_f32 v113, v112\n"
    "s_nop 0\n"
    "v_mul_f32 v13, v111, v113\n"
    "v_fma_f32 v111, -v13, v112, v111\n"
    "v_fma_f32 v111, v111, v113, v13\n"
    "v_cvt_pk_bf16_f32 v111, v111, 0\n"
    ".Lsg_138_%=:\n"
    "ds_write2_b32 v126, v100, v101 offset0:0 offset1:2\n"
    "ds_write2_b32 v126, v102, v103 offset0:4 offset1:6\n"
    "ds_write2_b32 v118, v104, v105 offset0:0 offset1:2\n"
    "ds_write2_b32 v118, v106, v107 offset0:4 offset1:6\n"
    "s_mov_b64 exec, 0xffff\n"
    "ds_write2_b32 v119, v108, v109 offset0:0 offset1:2\n"
    "ds_write2_b32 v119, v110, v111 offset0:4 offset1:6\n"
    "s_mov_b64 exec, -1\n"
    "s_mov_b32 s60, 4194304\n"
    "v_mov_b32 v12, 0\n"
    ".Llw_153_%=:\n"
    "ds_read_b32 v13, v12 offset:652\n"
    "s_waitcnt lgkmcnt(0)\n"
    "v_cmp_eq_u32 vcc, 8, v13\n"
    "s_cbranch_vccnz .Llwd_154_%=\n"
    "s_sleep 2\n"
    "s_sub_u32 s60, s60, 1\n"
    "s_cbranch_scc0 .Llw_153_%=\n"
    ".Llwd_154_%=:\n"
    ".Lsg2_155_%=:\n"
    "v_add_u32 v116, 23040, v125\n"
    "v_and_b32 v117, 48, v1\n"
    "s_cmp_eq_u32 s95, 2\n"
    "s_cselect_b32 s57, 1152, 0\n"
    "s_add_u32 s57, s57, 18176\n"
    "v_add_u32 v117, s57, v117\n"
    "v_bfe_u32 v12, v1, 2, 1\n"
    "v_mul_u32_u24 v12, 2432, v12\n"
    "v_add_u32 v117, v117, v12\n"
    "v_mov_b32 v160, 0\n"
    "v_mov_b32 v161, 0\n"
    "v_mov_b32 v162, 0\n"
    "v_mov_b32 v163, 0\n"
    "ds_read2st64_b64 v[16:19], v116 offset0:0 offset1:1\n"
    "ds_read2st64_b64 v[20:23], v116 offset0:2 offset1:3\n"
    "ds_read_b128 v[24:27], v117 offset:0\n"
    "ds_read2st64_b64 v[28:31], v116 offset0:4 offset1:5\n"
    "ds_read2st64_b64 v[32:35], v116 offset0:6 offset1:7\n"
    "ds_read_b128 v[36:39], v117 offset:64\n"
    "ds_read2st64_b64 v[40:43], v116 offset0:8 offset1:9\n"
    "ds_read2st64_b64 v[44:47], v116 offset0:10 offset1:11\n"
    "ds_read_b128 v[48:51], v117 offset:128\n"
    "ds_read2st64_b64 v[52:55], v116 offset0:12 offset1:13\n"
    "ds_read2st64_b64 v[56:59], v116 offset0:14 offset1:15\n"
    "ds_read_b128 v[60:63], v117 offset:192\n"
    "ds_read2st64_b64 v[64:67], v116 offset0:16 offset1:17\n"
    "ds_read2st64_b64 v[68:71], v116 offset0:18 offset1:19\n"
    "ds_read_b128 v[72:75], v117 offset:256\n"
    "s_waitcnt lgkmcnt(12)\n"
    "v_smfmac_f32_16x16x64_bf16 v[160:163], v[24:27], v[16:23], v4\n"
    "ds_read2st64_b64 v[16:19], v116 offset0:20 offset1:21\n"
    "ds_read2st64_b64 v[20:23], v116 offset0:22 offset1:23\n"
    "ds_read_b128 v[24:27], v117 offset:320\n"
    "s_waitcnt lgkmcnt(12)\n"
    "v_smfmac_f32_16x16x64_bf16 v[160:163], v[36:39], v[28:35], v4\n"
    "ds_read2st64_b64 v[28:31], v116 offset0:24 offset1:25\n"
    "ds_read2st64_b64 v[32:35], v116 offset0:26 offset1:27\n"
    "ds_read_b128 v[36:39], v117 offset:384\n"
    "s_waitcnt lgkmcnt(12)\n"
    "v_smfmac_f32_16x16x64_bf16 v[160:163], v[48:51], v[40:47], v4\n"
    "ds_read2st64_b64 v[40:43], v116 offset0:28 offset1:29\n"
    "ds_read2st64_b64 v[44:47], v116 offset0:30 offset1:31\n"
    "ds_read_b128 v[48:51], v117 offset:448\n"
    "s_waitcnt lgkmcnt(12)\n"
    "v_smfmac_f32_16x16x64_bf16 v[160:163], v[60:63], v[52:59], v4\n"
    "ds_read2st64_b64 v[52:55], v116 offset0:32 offset1:33\n"
    "ds_read2st64_b64 v[56:59], v116 offset0:34 offset1:35\n"
    "ds_read_b128 v[60:63], v117 offset:512\n"
    "s_waitcnt lgkmcnt(12)\n"
    "v_smfmac_f32_16x16x64_bf16 v[160:163], v[72:75], v[64:71], v4\n"
    "ds_read2st64_b64 v[64:67], v116 offset0:36 offset1:37\n"
    "ds_read2st64_b64 v[68:71], v116 offset0:38 offset1:39\n"
    "ds_read_b128 v[72:75], v117 offset:576\n"
    "s_waitcnt lgkmcnt(12)\n"
    "v_smfmac_f32_16x16x64_bf16 v[160:163], v[24:27], v[16:23], v4\n"
    "ds_read2st64_b64 v[16:19], v116 offset0:40 offset1:41\n"
    "ds_read2st64_b64 v[20:23], v116 offset0:42 offset1:43\n"
    "ds_read_b128 v[24:27], v117 offset:640\n"
    "s_waitcnt lgkmcnt(12)\n"
    "v_smfmac_f32_16x16x64_bf16 v[160:163], v[36:39], v[28:35], v4\n"
    "ds_read2st64_b64 v[28:31], v116 offset0:44 offset1:45\n"
    "ds_read2st64_b64 v[32:35], v116 offset0:46 offset1:47\n"
    "ds_read_b128 v[36:39], v117 offset:704\n"
    "s_waitcnt lgkmcnt(12)\n"
    "v_smfmac_f32_16x16x64_bf16 v[160:163], v[48:51], v[40:47], v4\n"
    "ds_read2st64_b64 v[40:43], v116 offset0:48 offset1:49\n"
    "ds_read2st64_b64 v[44:47], v116 offset0:50 offset1:51\n"
    "ds_read_b128 v[48:51], v117 offset:768\n"
    "s_waitcnt lgkmcnt(12)\n"
    "v_smfmac_f32_16x16x64_bf16 v[160:163], v[60:63], v[52:59], v4\n"
    "ds_read2st64_b64 v[52:55], v116 offset0:52 offset1:53\n"
    "ds_read2st64_b64 v[56:59], v116 offset0:54 offset1:55\n"
    "ds_read_b128 v[60:63], v117 offset:832\n"
    "s_waitcnt lgkmcnt(12)\n"
    "v_smfmac_f32_16x16x64_bf16 v[160:163], v[72:75], v[64:71], v4\n"
    "ds_read2st64_b64 v[64:67], v116 offset0:56 offset1:57\n"
    "ds_read2st64_b64 v[68:71], v116 offset0:58 offset1:59\n"
    "ds_read_b128 v[72:75], v117 offset:896\n"
    "s_waitcnt lgkmcnt(12)\n"
    "v_smfmac_f32_16x16x64_bf16 v[160:163], v[24:27], v[16:23], v4\n"
    "ds_read2st64_b64 v[16:19], v116 offset0:60 offset1:61\n"
    "ds_read2st64_b64 v[20:23], v116 offset0:62 offset1:63\n"
    "ds_read_b128 v[24:27], v117 offset:960\n"
    "s_waitcnt lgkmcnt(12)\n"
    "v_smfmac_f32_16x16x64_bf16 v[160:163], v[36:39], v[28:35], v4\n"
    "ds_read2st64_b64 v[28:31], v116 offset0:64 offset1:65\n"
    "ds_read2st64_b64 v[32:35], v116 offset0:66 offset1:67\n"
    "ds_read_b128 v[36:39], v117 offset:1024\n"
    "s_waitcnt lgkmcnt(12)\n"
    "v_smfmac_f32_16x16x64_bf16 v[160:163], v[48:51], v[40:47], v4\n"
    "ds_read2st64_b64 v[40:43], v116 offset0:68 offset1:69\n"
    "ds_read2st64_b64 v[44:47], v116 offset0:70 offset1:71\n"
    "ds_read_b128 v[48:51], v117 offset:1088\n"
    "s_waitcnt lgkmcnt(12)\n"
    "v_smfmac_f32_16x16x64_bf16 v[160:163], v[60:63], v[52:59], v4\n"
    "ds_read2st64_b64 v[52:55], v116 offset0:72 offset1:73\n"
    "ds_read2st64_b64 v[56:59], v116 offset0:74 offset1:75\n"
    "ds_read_b128 v[60:63], v117 offset:1152\n"
    "s_waitcnt lgkmcnt(12)\n"
    "v_smfmac_f32_16x16x64_bf16 v[160:163], v[72:75], v[64:71], v4\n"
    "ds_read2st64_b64 v[64:67], v116 offset0:76 offset1:77\n"
    "ds_read2st64_b64 v[68:71], v116 offset0:78 offset1:79\n"
    "ds_read_b128 v[72:75], v117 offset:1216\n"
    "s_waitcnt lgkmcnt(12)\n"
    "v_smfmac_f32_16x16x64_bf16 v[160:163], v[24:27], v[16:23], v4\n"
    "ds_read2st64_b64 v[16:19], v116 offset0:80 offset1:81\n"
    "ds_read2st64_b64 v[20:23], v116 offset0:82 offset1:83\n"
    "ds_read_b128 v[24:27], v117 offset:1280\n"
    "s_waitcnt lgkmcnt(12)\n"
    "v_smfmac_f32_16x16x64_bf16 v[160:163], v[36:39], v[28:35], v4\n"
    "ds_read2st64_b64 v[28:31], v116 offset0:84 offset1:85\n"
    "ds_read2st64_b64 v[32:35], v116 offset0:86 offset1:87\n"
    "ds_read_b128 v[36:39], v117 offset:1344\n"
    "s_waitcnt lgkmcnt(12)\n"
    "v_smfmac_f32_16x16x64_bf16 v[160:163], v[48:51], v[40:47], v4\n"
    "s_cmp_lg_u32 s95, 0\n"
    "s_cbranch_scc1 .Lgh_157_%=\n"
    "ds_read2st64_b64 v[40:43], v116 offset0:88 offset1:89\n"
    "ds_read2st64_b64 v[44:47], v116 offset0:90 offset1:91\n"
    "ds_read_b128 v[48:51], v117 offset:1408\n"
    "s_waitcnt lgkmcnt(12)\n"
    "v_smfmac_f32_16x16x64_bf16 v[160:163], v[60:63], v[52:59], v4\n"
    "ds_read2st64_b64 v[52:55], v116 offset0:92 offset1:93\n"
    "ds_read2st64_b64 v[56:59], v116 offset0:94 offset1:95\n"
    "ds_read_b128 v[60:63], v117 offset:1472\n"
    "s_waitcnt lgkmcnt(12)\n"
    "v_smfmac_f32_16x16x64_bf16 v[160:163], v[72:75], v[64:71], v4\n"
    "ds_read2st64_b64 v[64:67], v116 offset0:96 offset1:97\n"
    "ds_read2st64_b64 v[68:71], v116 offset0:98 offset1:99\n"
    "ds_read_b128 v[72:75], v117 offset:1536\n"
    "s_waitcnt lgkmcnt(12)\n"
    "v_smfmac_f32_16x16x64_bf16 v[160:163], v[24:27], v[16:23], v4\n"
    "ds_read2st64_b64 v[16:19], v116 offset0:100 offset1:101\n"
    "ds_read2st64_b64 v[20:23], v116 offset0:102 offset1:103\n"
    "ds_read_b128 v[24:27], v117 offset:1600\n"
    "s_waitcnt lgkmcnt(12)\n"
    "v_smfmac_f32_16x16x64_bf16 v[160:163], v[36:39], v[28:35], v4\n"
    "ds_read2st64_b64 v[28:31], v116 offset0:104 offset1:105\n"
    "ds_read2st64_b64 v[32:35], v116 offset0:106 offset1:107\n"
    "ds_read_b128 v[36:39], v117 offset:1664\n"
    "s_waitcnt lgkmcnt(12)\n"
    "v_smfmac_f32_16x16x64_bf16 v[160:163], v[48:51], v[40:47], v4\n"
    "ds_read2st64_b64 v[40:43], v116 offset0:108 offset1:109\n"
    "ds_read2st64_b64 v[44:47], v116 offset0:110 offset1:111\n"
    "ds_read_b128 v[48:51], v117 offset:1728\n"
    "s_waitcnt lgkmcnt(12)\n"
    "v_smfmac_f32_16x16x64_bf16 v[160:163], v[60:63], v[52:59], v4\n"
    "ds_read2st64_b64 v[52:55], v116 offset0:112 offset1:113\n"
    "ds_read2st64_b64 v[56:59], v116 offset0:114 offset1:115\n"
    "ds_read_b128 v[60:63], v117 offset:1792\n"
    "s_waitcnt lgkmcnt(12)\n"
    "v_smfmac_f32_16x16x64_bf16 v[160:163], v[72:75], v[64:71], v4\n"
    "ds_read2st64_b64 v[64:67], v116 offset0:116 offset1:117\n"
    "ds_read2st64_b64 v[68:71], v116 offset0:118 offset1:119\n"
    "ds_read_b128 v[72:75], v117 offset:1856\n"
    "s_waitcnt lgkmcnt(12)\n"
    "v_smfmac_f32_16x16x64_bf16 v[160:163], v[24:27], v[16:23], v4\n"
    "ds_read2st64_b64 v[16:19], v116 offset0:120 offset1:121\n"
    "ds_read2st64_b64 v[20:23], v116 offset0:122 offset1:123\n"
    "ds_read_b128 v[24:27], v117 offset:1920\n"
    "s_waitcnt lgkmcnt(12)\n"
    "v_smfmac_f32_16x16x64_bf16 v[160:163], v[36:39], v[28:35], v4\n"
    "ds_read2st64_b64 v[28:31], v116 offset0:124 offset1:125\n"
    "ds_read2st64_b64 v[32:35], v116 offset0:126 offset1:127\n"
    "ds_read_b128 v[36:39], v117 offset:1984\n"
    "s_waitcnt lgkmcnt(12)\n"
    "v_smfmac_f32_16x16x64_bf16 v[160:163], v[48:51], v[40:47], v4\n"
    "ds_read2st64_b64 v[40:43], v116 offset0:128 offset1:129\n"
    "ds_read2st64_b64 v[44:47], v116 offset0:130 offset1:131\n"
    "ds_read_b128 v[48:51], v117 offset:2048\n"
    "s_waitcnt lgkmcnt(12)\n"
    "v_smfmac_f32_16x16x64_bf16 v[160:163], v[60:63], v[52:59], v4\n"
    "ds_read2st64_b64 v[52:55], v116 offset0:132 offset1:133\n"
    "ds_read2st64_b64 v[56:59], v116 offset0:134 offset1:135\n"
    "ds_read_b128 v[60:63], v117 offset:2112\n"
    "s_waitcnt lgkmcnt(12)\n"
    "v_smfmac_f32_16x16x64_bf16 v[160:163], v[72:75], v[64:71], v4\n"
    "ds_read2st64_b64 v[64:67], v116 offset0:136 offset1:137\n"
    "ds_read2st64_b64 v[68:71], v116 offset0:138 offset1:139\n"
    "ds_read_b128 v[72:75], v117 offset:2176\n"
    "s_waitcnt lgkmcnt(12)\n"
    "v_smfmac_f32_16x16x64_bf16 v[160:163], v[24:27], v[16:23], v4\n"
    "ds_read2st64_b64 v[16:19], v116 offset0:140 offset1:141\n"
    "ds_read2st64_b64 v[20:23], v116 offset0:142 offset1:143\n"
    "ds_read_b128 v[24:27], v117 offset:2240\n"
    "s_waitcnt lgkmcnt(12)\n"
    "v_smfmac_f32_16x16x64_bf16 v[160:163], v[36:39], v[28:35], v4\n"
    "s_waitcnt lgkmcnt(9)\n"
    "v_smfmac_f32_16x16x64_bf16 v[160:163], v[48:51], v[40:47], v4\n"
    "s_waitcnt lgkmcnt(6)\n"
    "v_smfmac_f32_16x16x64_bf16 v[160:163], v[60:63], v[52:59], v4\n"
    "s_waitcnt lgkmcnt(3)\n"
    "v_smfmac_f32_16x16x64_bf16 v[160:163], v[72:75], v[64:71], v4\n"
    "s_waitcnt lgkmcnt(0)\n"
    "v_smfmac_f32_16x16x64_bf16 v[160:163], v[24:27], v[16:23], v4\n"
    "s_nop 5\n"
    "s_cmp_eq_u32 s53, 1\n"
    "s_cbranch_scc1 .Lgm_159_%=\n"
    ".Lgp_158_%=:\n"
    "v_mov_b32 v89, s54\n"
    "v_mov_b32 v91, s54\n"
    "v_cvt_pk_bf16_f32 v88, v160, v161\n"
    "v_cvt_pk_bf16_f32 v90, v162, v163\n"
    "s_mov_b64 exec, 0xffff\n"
    "buffer_store_dwordx4 v[88:91], v6, s[20:23], s77 offen sc1\n"
    "s_mov_b64 exec, -1\n"
    "s_branch .Lsq_140_%=\n"
    ".Lgm_159_%=:\n"
    "s_mov_b32 s88, -1\n"
    "s_mov_b32 s89, 0\n"
    "s_mov_b64 exec, s[88:89]\n"
    "ds_write_b128 v6, v[160:163] offset:17664\n"
    "s_mov_b64 exec, -1\n"
    "s_branch .Lrd_156_%=\n"
    ".Lgh_157_%=:\n"
    "s_waitcnt lgkmcnt(0)\n"
    "s_lshl1_add_u32 s76, s49, s95\n"
    "s_lshl_b32 s76, s76, 9\n"
    "s_add_u32 s76, s76, 704000\n"
    "v_and_b32 v117, 15, v1\n"
    "v_lshlrev_b32 v117, 5, v117\n"
    "s_nop 3\n"
    "v_mov_b32 v88, v160\n"
    "v_mov_b32 v89, s54\n"
    "v_mov_b32 v90, v161\n"
    "v_mov_b32 v91, s54\n"
    "v_mov_b32 v92, v162\n"
    "v_mov_b32 v93, s54\n"
    "v_mov_b32 v94, v163\n"
    "v_mov_b32 v95, s54\n"
    "s_mov_b64 exec, 0xffff\n"
    "buffer_store_dwordx4 v[88:91], v117, s[20:23], s76 offen sc1\n"
    "buffer_store_dwordx4 v[92:95], v117, s[20:23], s76 offen offset:16 sc1\n"
    "s_mov_b64 exec, -1\n"
    "s_branch .Lsq_140_%=\n"
    ".Lsn_139_%=:\n"
    "s_mov_b32 s60, 4194304\n"
    "v_mov_b32 v12, 0\n"
    ".Llw_160_%=:\n"
    "ds_read_b32 v13, v12 offset:692\n"
    "s_waitcnt lgkmcnt(0)\n"
    "v_cmp_ne_u32 vcc, 0, v13\n"
    "s_cbranch_vccnz .Llwd_161_%=\n"
    "s_sleep 2\n"
    "s_sub_u32 s60, s60, 1\n"
    "s_cbranch_scc0 .Llw_160_%=\n"
    ".Llwd_161_%=:\n"
    "v_readfirstlane_b32 s62, v13\n"
    "s_waitcnt vmcnt(0) lgkmcnt(0)\n"
    "v_readfirstlane_b32 s54, v124\n"
    "s_nop 0\n"
    "s_add_u32 s54, s54, 1\n"
    "s_cmp_eq_u32 s54, 0\n"
    "s_cselect_b32 s54, 1, s54\n"
    ".Lsq_140_%=:\n"
    "s_lshl_b32 s77, s51, 5\n"
    "s_add_u32 s77, s77, s52\n"
    "s_lshl_b32 s77, s77, 9\n"
    "s_add_u32 s77, s77, 73728\n"
    "s_lshl4_add_u32 s78, s51, s52\n"
    "s_lshl_b32 s78, s78, 7\n"
    "s_add_u32 s78, s78, 335872\n"
    "v_lshlrev_b32 v12, 7, v1\n"
    "v_bfe_u32 v13, v1, 2, 1\n"
    "v_mul_u32_u24 v13, 7680, v13\n"
    "v_add_u32 v13, v13, v12\n"
    "s_mov_b64 exec, 0xff\n"
    "buffer_load_dword v156, v13, s[20:23], s77 offen sc1\n"
    "s_mov_b64 exec, 1\n"
    "buffer_load_dword v157, v12, s[20:23], s78 offen sc1\n"
    "s_cmp_gt_u32 s53, 1\n"
    "s_cbranch_scc1 .Lx1_163_%=\n"
    "s_mul_i32 s77, s49, 2304\n"
    "s_lshl_b32 s57, s50, 8\n"
    "s_add_u32 s77, s77, s57\n"
    "s_add_u32 s77, s77, 552960\n"
    "s_mov_b64 exec, -1\n"
    "v_lshrrev_b32 v13, 1, v1\n"
    "v_mul_u32_u24 v13, 18176, v13\n"
    "v_add_u32 v13, v13, v12\n"
    "s_mov_b64 exec, 255\n"
    "s_cmp_eq_u32 s53, 0\n"
    "s_cbranch_scc1 .Lx0_162_%=\n"
    "s_mul_i32 s77, s56, 2304\n"
    "s_add_u32 s77, s77, 387072\n"
    "s_mov_b64 exec, 0x3ffff\n"
    "v_mov_b32 v13, v12\n"
    ".Lx0_162_%=:\n"
    "buffer_load_dword v158, v13, s[20:23], s77 offen sc1\n"
    ".Lx1_163_%=:\n"
    "s_mov_b64 exec, -1\n"
    "s_cmp_gt_u32 s2, 15\n"
    "s_cbranch_scc1 .Lc0_164_%=\n"
    "s_mov_b32 s60, 4194304\n"
    "v_mov_b32 v12, 0\n"
    ".Llw_165_%=:\n"
    "ds_read_b32 v13, v12 offset:672\n"
    "s_waitcnt lgkmcnt(0)\n"
    "v_cmp_eq_u32 vcc, 8, v13\n"
    "s_cbranch_vccnz .Llwd_166_%=\n"
    "s_sleep 2\n"
    "s_sub_u32 s60, s60, 1\n"
    "s_cbranch_scc0 .Llw_165_%=\n"
    ".Llwd_166_%=:\n"
    "s_memrealtime s[90:91]\n"
    "s_lshl_b32 s79, s2, 7\n"
    "s_add_u32 s79, s79, 6144\n"
    "v_mov_b32 v145, s54\n"
    "v_mov_b32 v147, s54\n"
    "s_waitcnt lgkmcnt(0)\n"
    "v_mov_b32 v144, s90\n"
    "s_sub_u32 s90, s90, s62\n"
    "v_mov_b32 v146, s90\n"
    "s_mov_b64 exec, 1\n"
    "buffer_store_dwordx4 v[144:147], off, s[20:23], s79 sc1\n"
    "s_mov_b64 exec, -1\n"
    ".Lc0_164_%=:\n"
    "v_and_b32 v114, 15, v1\n"
    "v_lshlrev_b32 v114, 7, v114\n"
    "s_lshl_b32 s74, s51, 10\n"
    "s_add_u32 s74, s74, 57344\n"
    "s_lshl_b32 s75, s49, 10\n"
    "s_add_u32 s75, s75, 704512\n"
    "s_lshr_b32 s61, s24, 7\n"
    "s_sub_u32 s61, 3, s61\n"
    "s_and_b32 s61, s61, 7\n"
    "s_and_b32 s57, s51, 1\n"
    "s_lshl_b32 s57, s57, 2\n"
    "s_sub_u32 s73, s61, s57\n"
    "v_lshrrev_b32 v113, 4, v1\n"
    "v_cmp_ne_u32 vcc, s73, v113\n"
    "s_nop 1\n"
    "v_cndmask_b32 v113, 0, -1, vcc\n"
    "v_and_b32 v117, 15, v1\n"
    "v_lshlrev_b32 v117, 4, v117\n"
    "s_cmp_lg_u32 s53, 1\n"
    "s_cbranch_scc1 .Lnx_167_%=\n"
    "buffer_load_dwordx2 v[148:149], v125, s[28:31], 0 offen\n"
    "buffer_load_dwordx2 v[150:151], v125, s[28:31], 0 offen offset:512\n"
    "buffer_load_dwordx2 v[152:153], v125, s[28:31], 0 offen offset:1024\n"
    ".Lnx_167_%=:\n"
    ".Ltw_168_%=:\n"
    "s_memrealtime s[90:91]\n"
    "s_waitcnt lgkmcnt(0)\n"
    "s_sub_u32 s90, s90, s62\n"
    "s_cmp_ge_u32 s90, 680\n"
    "s_cbranch_scc1 .Ltwd_169_%=\n"
    "s_sleep 1\n"
    "s_branch .Ltw_168_%=\n"
    ".Ltwd_169_%=:\n"
    "s_mov_b32 s60, 65536\n"
    ".Lcp_170_%=:\n"
    "s_mov_b32 s79, 6144\n"
    "buffer_load_dwordx4 v[144:147], v114, s[20:23], s79 offen sc1\n"
    "s_waitcnt vmcnt(0)\n"
    "v_xor_b32 v12, s54, v145\n"
    "v_xor_b32 v13, s54, v147\n"
    "v_or_b32 v12, v12, v13\n"
    "v_cmp_eq_u32 vcc, 0, v12\n"
    "v_subrev_u32 v144, s62, v144\n"
    "v_mov_b32 v13, 0x100000\n"
    "v_cndmask_b32 v144, v13, v144, vcc\n"
    "v_cndmask_b32 v146, v13, v146, vcc\n"
    "s_nop 0\n"
    "v_min_i32_dpp v144, v144, v144 row_ror:8 row_mask:0xf bank_mask:0xf bound_ctrl:1\n"
    "v_min_i32_dpp v146, v146, v146 row_ror:8 row_mask:0xf bank_mask:0xf bound_ctrl:1\n"
    "v_cmp_eq_u32 vcc, v13, v144\n"
    "s_cbranch_vccz .Lcpd_171_%=\n"
    "s_sleep 12\n"
    "s_sub_u32 s60, s60, 1\n"
    "s_cbranch_scc0 .Lcp_170_%=\n"
    "v_mov_b32 v144, 0\n"
    ".Lcpd_171_%=:\n"
    "v_add_u32 v144, s62, v144\n"
    "v_subrev_u32 v144, s62, v144\n"
    "v_subrev_u32 v147, 20, v144\n"
    "s_nop 1\n"
    "v_max_i32_dpp v147, v147, v147 quad_perm:[1,0,3,2] row_mask:0xf bank_mask:0xf bound_ctrl:1\n"
    "s_nop 1\n"
    "v_max_i32_dpp v147, v147, v147 quad_perm:[2,3,0,1] row_mask:0xf bank_mask:0xf bound_ctrl:1\n"
    "s_nop 1\n"
    "v_max_i32_dpp v147, v147, v147 row_half_mirror row_mask:0xf bank_mask:0xf bound_ctrl:1\n"
    "s_nop 1\n"
    "v_max_i32_dpp v147, v147, v147 row_ror:8 row_mask:0xf bank_mask:0xf bound_ctrl:1\n"
    "v_add_u32_dpp v144, v144, v144 quad_perm:[1,0,3,2] row_mask:0xf bank_mask:0xf bound_ctrl:1\n"
    "v_add_u32_dpp v146, v146, v146 quad_perm:[1,0,3,2] row_mask:0xf bank_mask:0xf bound_ctrl:1\n"
    "s_nop 0\n"
    "v_add_u32_dpp v144, v144, v144 quad_perm:[2,3,0,1] row_mask:0xf bank_mask:0xf bound_ctrl:1\n"
    "v_add_u32_dpp v146, v146, v146 quad_perm:[2,3,0,1] row_mask:0xf bank_mask:0xf bound_ctrl:1\n"
    "s_nop 0\n"
    "v_add_u32_dpp v144, v144, v144 row_half_mirror row_mask:0xf bank_mask:0xf bound_ctrl:1\n"
    "v_add_u32_dpp v146, v146, v146 row_half_mirror row_mask:0xf bank_mask:0xf bound_ctrl:1\n"
    "s_nop 0\n"
    "v_add_u32_dpp v144, v144, v144 row_ror:8 row_mask:0xf bank_mask:0xf bound_ctrl:1\n"
    "v_add_u32_dpp v146, v146, v146 row_ror:8 row_mask:0xf bank_mask:0xf bound_ctrl:1\n"
    "v_ashrrev_i32 v144, 4, v144\n"
    "v_max_i32 v144, v144, v147\n"
    "v_add_u32 v144, s62, v144\n"
    "v_subrev_u32 v144, 550, v144\n"
    "v_or_b32 v144, 1, v144\n"
    "v_mov_b32 v12, 0\n"
    "v_mov_b32 v145, 0\n"
    "v_mov_b32 v146, v144\n"
    "ds_write_b32 v12, v146 offset:688\n"
    "ds_write_b64 v12, v[144:145] offset:680\n"
    "v_readfirstlane_b32 s63, v146\n"
    ".Ltw_172_%=:\n"
    "s_memrealtime s[90:91]\n"
    "s_waitcnt lgkmcnt(0)\n"
    "s_sub_u32 s90, s90, s62\n"
    "s_cmp_ge_u32 s90, 780\n"
    "s_cbranch_scc1 .Ltwd_173_%=\n"
    "s_sleep 1\n"
    "s_branch .Ltw_172_%=\n"
    ".Ltwd_173_%=:\n"
    "s_mov_b32 s60, 65536\n"
    ".Lqp_174_%=:\n"
    "buffer_load_dwordx4 v[88:91], v6, s[20:23], s74 offen sc1\n"
    "buffer_load_dwordx4 v[92:95], v6, s[20:23], s75 offen sc1\n"
    "s_cmp_lg_u32 s53, 1\n"
    "s_cbranch_scc1 .Lqp1_176_%=\n"
    "s_mov_b32 s76, 48128\n"
    "buffer_load_dwordx4 v[16:19], v6, s[20:23], s76 offen sc1\n"
    "buffer_load_dwordx4 v[20:23], v6, s[20:23], s76 offen offset:1024 sc1\n"
    "buffer_load_dwordx4 v[24:27], v117, s[20:23], s76 offen offset:2048 sc1\n"
    "s_mov_b32 s76, 50432\n"
    "buffer_load_dwordx4 v[28:31], v6, s[20:23], s76 offen sc1\n"
    "buffer_load_dwordx4 v[32:35], v6, s[20:23], s76 offen offset:1024 sc1\n"
    "buffer_load_dwordx4 v[36:39], v117, s[20:23], s76 offen offset:2048 sc1\n"
    "s_mov_b32 s76, 52736\n"
    "buffer_load_dwordx4 v[40:43], v6, s[20:23], s76 offen sc1\n"
    "buffer_load_dwordx4 v[44:47], v6, s[20:23], s76 offen offset:1024 sc1\n"
    "buffer_load_dwordx4 v[48:51], v117, s[20:23], s76 offen offset:2048 sc1\n"
    ".Lqp1_176_%=:\n"
    "s_waitcnt vmcnt(0)\n"
    "v_xor_b32 v12, s54, v89\n"
    "v_xor_b32 v13, s54, v91\n"
    "v_or_b32 v12, v12, v13\n"
    "v_and_b32 v12, v12, v113\n"
    "v_xor_b32 v13, s54, v93\n"
    "v_or_b32 v12, v12, v13\n"
    "v_xor_b32 v13, s54, v95\n"
    "v_or_b32 v12, v12, v13\n"
    "s_cbranch_scc1 .Lqp2_177_%=\n"
    "v_xor_b32 v13, s54, v17\n"
    "v_or_b32 v12, v12, v13\n"
    "v_xor_b32 v13, s54, v19\n"
    "v_or_b32 v12, v12, v13\n"
    "v_xor_b32 v13, s54, v21\n"
    "v_or_b32 v12, v12, v13\n"
    "v_xor_b32 v13, s54, v23\n"
    "v_or_b32 v12, v12, v13\n"
    "v_xor_b32 v13, s54, v25\n"
    "v_or_b32 v12, v12, v13\n"
    "v_xor_b32 v13, s54, v27\n"
    "v_or_b32 v12, v12, v13\n"
    "v_xor_b32 v13, s54, v29\n"
    "v_or_b32 v12, v12, v13\n"
    "v_xor_b32 v13, s54, v31\n"
    "v_or_b32 v12, v12, v13\n"
    "v_xor_b32 v13, s54, v33\n"
    "v_or_b32 v12, v12, v13\n"
    "v_xor_b32 v13, s54, v35\n"
    "v_or_b32 v12, v12, v13\n"
    "v_xor_b32 v13, s54, v37\n"
    "v_or_b32 v12, v12, v13\n"
    "v_xor_b32 v13, s54, v39\n"
    "v_or_b32 v12, v12, v13\n"
    "v_xor_b32 v13, s54, v41\n"
    "v_or_b32 v12, v12, v13\n"
    "v_xor_b32 v13, s54, v43\n"
    "v_or_b32 v12, v12, v13\n"
    "v_xor_b32 v13, s54, v45\n"
    "v_or_b32 v12, v12, v13\n"
    "v_xor_b32 v13, s54, v47\n"
    "v_or_b32 v12, v12, v13\n"
    "v_xor_b32 v13, s54, v49\n"
    "v_or_b32 v12, v12, v13\n"
    "v_xor_b32 v13, s54, v51\n"
    "v_or_b32 v12, v12, v13\n"
    ".Lqp2_177_%=:\n"
    "v_cmp_ne_u32 vcc, 0, v12\n"
    "s_cbranch_vccz .Lqpd_175_%=\n"
    "s_sleep 12\n"
    "s_sub_u32 s60, s60, 1\n"
    "s_cbranch_scc0 .Lqp_174_%=\n"
    ".Lqpd_175_%=:\n"
    "v_mov_b32 v89, v90\n"
    "ds_write_b64 v125, v[88:89]\n"
    "s_cmp_gt_u32 s73, 3\n"
    "s_cbranch_scc1 .Lnh_178_%=\n"
    "v_mov_b32 v93, v92\n"
    "v_mov_b32 v95, v94\n"
    "s_lshl_b32 s73, s73, 7\n"
    "v_add_u32 v13, s73, v127\n"
    "v_permlane32_swap_b32 v92, v93\n"
    "v_permlane32_swap_b32 v94, v95\n"
    "v_add_f32 v92, v92, v93\n"
    "v_add_f32 v94, v94, v95\n"
    "v_cvt_pk_bf16_f32 v92, v92, v94\n"
    "s_mov_b32 s78, -1\n"
    "s_mov_b32 s79, 0\n"
    "s_mov_b64 exec, s[78:79]\n"
    "ds_write_b32 v13, v92\n"
    "s_mov_b64 exec, -1\n"
    ".Lnh_178_%=:\n"
    "v_mov_b32 v12, 0\n"
    "v_mov_b32 v13, s54\n"
    "ds_write_b32 v12, v13 offset:640\n"
    "s_waitcnt vmcnt(0)\n"
    "s_movk_i32 s93, 0x3ff\n"
    "s_and_b32 s92, s54, s93\n"
    "v_mov_b32 v0, s92\n"
    "s_cmp_eq_u32 s53, 1\n"
    "s_cbranch_scc1 .Ls4_137_%=\n"
    "s_cmp_lg_u32 s53, 3\n"
    "s_cbranch_scc1 .Lrd_156_%=\n"
    "v_and_b32 v50, 3, v1\n"
    "v_lshrrev_b32 v49, 4, v1\n"
    "v_lshl_add_u32 v48, v50, 2, v49\n"
    "s_lshl_b32 s57, s56, 4\n"
    "v_add_u32 v48, s57, v48\n"
    "v_lshlrev_b32 v49, 1, v48\n"
    "v_lshlrev_b32 v48, 2, v48\n"
    "v_lshlrev_b32 v50, 4, v50\n"
    "s_lshl_b32 s76, s56, 6\n"
    "s_add_u32 s76, s76, 48128\n"
    "s_add_u32 s77, s76, 6912\n"
    "buffer_load_ushort v51, v49, s[28:31], 0 offen\n"
    "s_mov_b32 s60, 65536\n"
    ".Ltp_179_%=:\n"
    "buffer_load_dwordx4 v[92:95], v50, s[20:23], s76 offen sc1\n"
    "buffer_load_dwordx4 v[96:99], v50, s[20:23], s77 offen sc1\n"
    "s_waitcnt vmcnt(0)\n"
    "v_xor_b32 v12, s54, v93\n"
    "v_xor_b32 v13, s54, v95\n"
    "v_or_b32 v12, v12, v13\n"
    "v_xor_b32 v13, s54, v97\n"
    "v_or_b32 v12, v12, v13\n"
    "v_xor_b32 v13, s54, v99\n"
    "v_or_b32 v12, v12, v13\n"
    "v_cmp_ne_u32 vcc, 0, v12\n"
    "s_cbranch_vccz .Ltpd_180_%=\n"
    "s_sleep 0\n"
    "s_sub_u32 s60, s60, 1\n"
    "s_cbranch_scc0 .Ltp_179_%=\n"
    ".Ltpd_180_%=:\n"
    "v_cmp_lt_u32 vcc, 31, v1\n"
    "v_lshlrev_b32 v14, 4, v10\n"
    "v_lshlrev_b32 v51, 16, v51\n"
    "v_cndmask_b32 v52, v92, v94, vcc\n"
    "v_lshrrev_b32 v52, v14, v52\n"
    "v_lshlrev_b32 v52, 16, v52\n"
    "v_cndmask_b32 v54, v96, v98, vcc\n"
    "v_lshrrev_b32 v54, v14, v54\n"
    "v_lshlrev_b32 v54, 16, v54\n"
    ".Lrd_156_%=:\n"
    "s_mov_b32 s60, 4194304\n"
    "v_mov_b32 v12, 0\n"
    ".Llw_181_%=:\n"
    "ds_read_b32 v13, v12 offset:644\n"
    "s_waitcnt lgkmcnt(0)\n"
    "v_cmp_eq_u32 vcc, 8, v13\n"
    "s_cbranch_vccnz .Llwd_182_%=\n"
    "s_sleep 2\n"
    "s_sub_u32 s60, s60, 1\n"
    "s_cbranch_scc0 .Llw_181_%=\n"
    ".Llwd_182_%=:\n"
    "v_lshlrev_b32 v12, 5, v8\n"
    "ds_read_b128 v[88:91], v12 offset:512\n"
    "ds_read_b128 v[92:95], v12 offset:528\n"
    "ds_read_b128 v[96:99], v12 offset:576\n"
    "ds_read_b128 v[100:103], v12 offset:592\n"
    "s_lshl4_add_u32 s76, s51, s52\n"
    "s_lshl_b32 s76, s76, 7\n"
    "s_add_u32 s76, s76, 335872\n"
    "s_lshl_b32 s77, s51, 5\n"
    "s_add_u32 s77, s77, s52\n"
    "s_lshl_b32 s77, s77, 9\n"
    "s_add_u32 s77, s77, 73728\n"
    "s_waitcnt lgkmcnt(0)\n"
    "v_max3_f32 v104, v88, v89, v90\n"
    "v_max3_f32 v104, v104, v91, v92\n"
    "v_max3_f32 v104, v104, v93, v94\n"
    "v_max_f32 v104, v104, v95\n"
    "v_sub_f32 v88, v88, v104\n"
    "v_sub_f32 v89, v89, v104\n"
    "v_sub_f32 v90, v90, v104\n"
    "v_sub_f32 v91, v91, v104\n"
    "v_sub_f32 v92, v92, v104\n"
    "v_sub_f32 v93, v93, v104\n"
    "v_sub_f32 v94, v94, v104\n"
    "v_sub_f32 v95, v95, v104\n"
    "v_mul_f32 v88, 0x3fb8aa3b, v88\n"
    "v_mul_f32 v89, 0x3fb8aa3b, v89\n"
    "v_mul_f32 v90, 0x3fb8aa3b, v90\n"
    "v_mul_f32 v91, 0x3fb8aa3b, v91\n"
    "v_mul_f32 v92, 0x3fb8aa3b, v92\n"
    "v_mul_f32 v93, 0x3fb8aa3b, v93\n"
    "v_mul_f32 v94, 0x3fb8aa3b, v94\n"
    "v_mul_f32 v95, 0x3fb8aa3b, v95\n"
    "v_exp_f32 v88, v88\n"
    "v_exp_f32 v89, v89\n"
    "v_exp_f32 v90, v90\n"
    "v_exp_f32 v91, v91\n"
    "v_exp_f32 v92, v92\n"
    "v_exp_f32 v93, v93\n"
    "v_exp_f32 v94, v94\n"
    "v_exp_f32 v95, v95\n"
    "v_mul_f32 v138, v88, v96\n"
    "v_mov_b32 v136, v104\n"
    "v_mov_b32 v137, s54\n"
    "v_mov_b32 v139, s54\n"
    "v_fmac_f32 v138, v89, v97\n"
    "v_fmac_f32 v138, v90, v98\n"
    "v_fmac_f32 v138, v91, v99\n"
    "v_fmac_f32 v138, v92, v100\n"
    "v_fmac_f32 v138, v93, v101\n"
    "v_fmac_f32 v138, v94, v102\n"
    "v_fmac_f32 v138, v95, v103\n"
    "v_lshlrev_b32 v119, 4, v8\n"
    "s_mov_b32 s78, 1\n"
    "s_mov_b32 s79, 1\n"
    "s_mov_b32 s69, 0\n"
    "s_memrealtime s[90:91]\n"
    "s_waitcnt lgkmcnt(0)\n"
    "s_and_b32 s90, s90, 0xffff\n"
    "v_or_b32 v136, s90, v104\n"
    "s_mov_b64 exec, s[78:79]\n"
    "buffer_store_dwordx4 v[136:139], v119, s[20:23], s76 offen sc1\n"
    "s_mov_b64 exec, -1\n"
    ".Lmlnow_183_%=:\n"
    "v_and_b32 v12, 15, v1\n"
    "v_lshlrev_b32 v118, 5, v12\n"
    "v_lshl_add_u32 v118, v10, 4, v118\n"
    "v_lshl_add_u32 v118, v8, 13, v118\n"
    "s_barrier\n"
    "ds_read_b128 v[32:35], v6 offset:1280\n"
    "ds_read_b128 v[36:39], v6 offset:2304\n"
    "ds_read_b128 v[40:43], v6 offset:3328\n"
    "ds_read_b128 v[44:47], v6 offset:4352\n"
    "ds_read_b128 v[96:99], v6 offset:5376\n"
    "ds_read_b128 v[100:103], v6 offset:6400\n"
    "ds_read_b128 v[112:115], v6 offset:7424\n"
    "ds_read_b128 v[108:111], v6 offset:8448\n"
    "s_waitcnt lgkmcnt(7)\n"
    "v_pk_mul_f32 v[140:141], v[32:33], v[88:89] op_sel_hi:[1,0]\n"
    "v_pk_mul_f32 v[142:143], v[34:35], v[88:89] op_sel_hi:[1,0]\n"
    "s_waitcnt lgkmcnt(6)\n"
    "v_pk_mul_f32 v[132:133], v[36:37], v[88:89] op_sel:[0,1] op_sel_hi:[1,1]\n"
    "v_pk_mul_f32 v[134:135], v[38:39], v[88:89] op_sel:[0,1] op_sel_hi:[1,1]\n"
    "s_waitcnt lgkmcnt(5)\n"
    "v_pk_fma_f32 v[140:141], v[40:41], v[90:91], v[140:141] op_sel_hi:[1,0,1]\n"
    "v_pk_fma_f32 v[142:143], v[42:43], v[90:91], v[142:143] op_sel_hi:[1,0,1]\n"
    "s_waitcnt lgkmcnt(4)\n"
    "v_pk_fma_f32 v[132:133], v[44:45], v[90:91], v[132:133] op_sel:[0,1,0] op_sel_hi:[1,1,1]\n"
    "v_pk_fma_f32 v[134:135], v[46:47], v[90:91], v[134:135] op_sel:[0,1,0] op_sel_hi:[1,1,1]\n"
    "s_waitcnt lgkmcnt(3)\n"
    "v_pk_fma_f32 v[140:141], v[96:97], v[92:93], v[140:141] op_sel_hi:[1,0,1]\n"
    "v_pk_fma_f32 v[142:143], v[98:99], v[92:93], v[142:143] op_sel_hi:[1,0,1]\n"
    "s_waitcnt lgkmcnt(2)\n"
    "v_pk_fma_f32 v[132:133], v[100:101], v[92:93], v[132:133] op_sel:[0,1,0] op_sel_hi:[1,1,1]\n"
    "v_pk_fma_f32 v[134:135], v[102:103], v[92:93], v[134:135] op_sel:[0,1,0] op_sel_hi:[1,1,1]\n"
    "s_waitcnt lgkmcnt(1)\n"
    "v_pk_fma_f32 v[140:141], v[112:113], v[94:95], v[140:141] op_sel_hi:[1,0,1]\n"
    "v_pk_fma_f32 v[142:143], v[114:115], v[94:95], v[142:143] op_sel_hi:[1,0,1]\n"
    "s_waitcnt lgkmcnt(0)\n"
    "v_pk_fma_f32 v[132:133], v[108:109], v[94:95], v[132:133] op_sel:[0,1,0] op_sel_hi:[1,1,1]\n"
    "v_pk_fma_f32 v[134:135], v[110:111], v[94:95], v[134:135] op_sel:[0,1,0] op_sel_hi:[1,1,1]\n"
    "v_pk_add_f32 v[140:141], v[140:141], v[132:133]\n"
    "v_pk_add_f32 v[142:143], v[142:143], v[134:135]\n"
    "v_bfi_b32 v140, s93, v0, v140\n"
    "v_bfi_b32 v141, s93, v0, v141\n"
    "v_bfi_b32 v142, s93, v0, v142\n"
    "v_bfi_b32 v143, s93, v0, v143\n"
    ".Lpub_185_%=:\n"
    "buffer_store_dwordx4 v[140:143], v118, s[20:23], s77 offen sc1\n"
    "s_cmp_eq_u32 s53, 3\n"
    "s_cbranch_scc1 .Lt3_188_%=\n"
    "s_cmp_eq_u32 s53, 1\n"
    "s_cbranch_scc1 .Lt1_187_%=\n"
    "s_cmp_eq_u32 s53, 0\n"
    "s_cbranch_scc1 .Lt0_186_%=\n"
    "s_endpgm\n"
    ".Lt1_187_%=:\n"
    "v_lshlrev_b32 v165, 4, v8\n"
    "v_lshl_add_u32 v165, v9, 3, v165\n"
    "v_add_u32 v165, v165, v10\n"
    "v_mul_u32_u24 v165, 1152, v165\n"
    "v_lshl_add_u32 v165, v11, 4, v165\n"
    "s_mul_i32 s70, s56, 73728\n"
    "s_lshl_b32 s57, s48, 7\n"
    "s_add_u32 s70, s70, s57\n"
    "s_setprio 0\n"
    "s_branch .LM2_126_%=\n"
    ".Lt0_186_%=:\n"
    "v_and_b32 v13, 15, v1\n"
    "v_lshlrev_b32 v55, 7, v13\n"
    "v_lshl_add_u32 v55, v10, 4, v55\n"
    "v_lshl_add_u32 v55, v8, 11, v55\n"
    "s_lshl_b32 s73, s49, 12\n"
    "s_add_u32 s73, s73, 335872\n"
    "v_and_b32 v56, 3, v1\n"
    "v_lshlrev_b32 v56, 4, v56\n"
    "v_and_b32 v57, 12, v1\n"
    "v_add_u32 v56, v56, v57\n"
    "v_and_b32 v57, 48, v1\n"
    "v_lshl_add_u32 v56, v57, 2, v56\n"
    "s_mov_b32 s60, 65536\n"
    ".Ltp_190_%=:\n"
    "buffer_load_dwordx4 v[50:53], v55, s[20:23], s73 offen sc1\n"
    "s_waitcnt vmcnt(0)\n"
    "v_xor_b32 v12, s54, v51\n"
    "v_xor_b32 v13, s54, v53\n"
    "v_or_b32 v12, v12, v13\n"
    "v_cmp_ne_u32 vcc, 0, v12\n"
    "s_cbranch_vccz .Ltpd_191_%=\n"
    "s_sleep 4\n"
    "s_sub_u32 s60, s60, 1\n"
    "s_cbranch_scc0 .Ltp_190_%=\n"
    ".Ltpd_191_%=:\n"
    "s_memrealtime s[90:91]\n"
    "s_waitcnt lgkmcnt(0)\n"
    "v_sub_u32 v58, s90, v50\n"
    "v_and_b32 v58, 0xffff, v58\n"
    "v_and_b32 v50, 0xffff0000, v50\n"
    "s_nop 0\n"
    "v_min_u32_dpp v58, v58, v58 quad_perm:[1,0,3,2] row_mask:0xf bank_mask:0xf bound_ctrl:1\n"
    "s_nop 1\n"
    "v_min_u32_dpp v58, v58, v58 quad_perm:[2,3,0,1] row_mask:0xf bank_mask:0xf bound_ctrl:1\n"
    "s_nop 1\n"
    "v_min_u32_dpp v58, v58, v58 row_half_mirror row_mask:0xf bank_mask:0xf bound_ctrl:1\n"
    "s_nop 1\n"
    "v_min_u32_dpp v58, v58, v58 row_ror:8 row_mask:0xf bank_mask:0xf bound_ctrl:1\n"
    "v_mov_b32 v59, v58\n"
    "s_nop 1\n"
    "v_permlane16_swap_b32 v58, v59\n"
    "v_min_u32 v58, v58, v59\n"
    "v_mov_b32 v59, v58\n"
    "s_nop 1\n"
    "v_permlane32_swap_b32 v58, v59\n"
    "v_min_u32 v58, v58, v59\n"
    "v_sub_u32 v58, s90, v58\n"
    "v_or_b32 v58, 1, v58\n"
    "v_mov_b32 v59, s54\n"
    "v_mov_b32 v51, v52\n"
    "v_mov_b32 v52, v50\n"
    "s_nop 1\n"
    "v_max_f32_dpp v52, v52, v52 quad_perm:[1,0,3,2] row_mask:0xf bank_mask:0xf bound_ctrl:1\n"
    "s_nop 1\n"
    "v_max_f32_dpp v52, v52, v52 quad_perm:[2,3,0,1] row_mask:0xf bank_mask:0xf bound_ctrl:1\n"
    "s_nop 1\n"
    "v_max_f32_dpp v52, v52, v52 row_half_mirror row_mask:0xf bank_mask:0xf bound_ctrl:1\n"
    "s_nop 1\n"
    "v_max_f32_dpp v52, v52, v52 row_ror:8 row_mask:0xf bank_mask:0xf bound_ctrl:1\n"
    "v_sub_f32 v52, v50, v52\n"
    "v_mul_f32 v52, 0x3fb8aa3b, v52\n"
    "v_exp_f32 v52, v52\n"
    "s_nop 0\n"
    "v_mul_f32 v53, v52, v51\n"
    "s_nop 1\n"
    "v_add_f32_dpp v53, v53, v53 quad_perm:[1,0,3,2] row_mask:0xf bank_mask:0xf bound_ctrl:1\n"
    "s_nop 1\n"
    "v_add_f32_dpp v53, v53, v53 quad_perm:[2,3,0,1] row_mask:0xf bank_mask:0xf bound_ctrl:1\n"
    "s_nop 1\n"
    "v_add_f32_dpp v53, v53, v53 row_half_mirror row_mask:0xf bank_mask:0xf bound_ctrl:1\n"
    "s_nop 1\n"
    "v_add_f32_dpp v53, v53, v53 row_ror:8 row_mask:0xf bank_mask:0xf bound_ctrl:1\n"
    "v_rcp_f32 v54, v53\n"
    "v_mov_b32 v12, 0\n"
    "v_mul_f32 v52, v52, v54\n"
    "ds_write_b32 v56, v52 offset:17664\n"
    "ds_write_b32 v12, v58 offset:664\n"
    ".Lwk_189_%=:\n"
    "v_lshrrev_b32 v12, 4, v1\n"
    "v_and_b32 v13, 15, v1\n"
    "v_lshlrev_b32 v88, 10, v12\n"
    "v_lshl_add_u32 v88, v13, 4, v88\n"
    "v_lshlrev_b32 v89, 4, v13\n"
    "v_lshl_add_u32 v89, v12, 2, v89\n"
    "s_mul_i32 s76, s49, 2304\n"
    "s_lshl_b32 s57, s50, 8\n"
    "s_add_u32 s76, s76, s57\n"
    "s_add_u32 s76, s76, 552960\n"
    "s_add_u32 s77, s76, 18432\n"
    "s_add_u32 s78, s76, 36864\n"
    "s_add_u32 s79, s76, 55296\n"
    "s_barrier\n"
    "ds_read_b128 v[92:95], v88 offset:9472\n"
    "ds_read_b128 v[96:99], v88 offset:9728\n"
    "ds_read_b128 v[100:103], v88 offset:9984\n"
    "ds_read_b128 v[104:107], v88 offset:10240\n"
    "s_waitcnt lgkmcnt(2)\n"
    "v_pk_add_f32 v[92:93], v[92:93], v[96:97]\n"
    "v_pk_add_f32 v[94:95], v[94:95], v[98:99]\n"
    "s_waitcnt lgkmcnt(0)\n"
    "v_pk_add_f32 v[100:101], v[100:101], v[104:105]\n"
    "v_pk_add_f32 v[102:103], v[102:103], v[106:107]\n"
    "v_pk_add_f32 v[92:93], v[92:93], v[100:101]\n"
    "v_pk_add_f32 v[94:95], v[94:95], v[102:103]\n"
    "s_nop 1\n"
    "v_permlane32_swap_b32 v92, v94\n"
    "v_permlane32_swap_b32 v93, v95\n"
    "v_add_f32 v92, v92, v94\n"
    "v_add_f32 v93, v93, v95\n"
    "s_nop 1\n"
    "v_permlane16_swap_b32 v92, v93\n"
    "v_add_f32 v92, v92, v93\n"
    "v_bfi_b32 v92, s93, v0, v92\n"
    "buffer_store_dword v92, v89, s[20:23], s76 offen sc1\n"
    "buffer_store_dword v92, v89, s[20:23], s77 offen sc1\n"
    "buffer_store_dword v92, v89, s[20:23], s78 offen sc1\n"
    "buffer_store_dword v92, v89, s[20:23], s79 offen sc1\n"
    "s_endpgm\n"
    ".Lt3_188_%=:\n"
    "v_lshrrev_b32 v12, 2, v1\n"
    "v_mul_u32_u24 v12, 2304, v12\n"
    "v_add_u32 v56, v50, v12\n"
    "v_lshrrev_b32 v12, 4, v1\n"
    "v_mul_u32_u24 v12, 2304, v12\n"
    "v_add_u32 v57, v50, v12\n"
    "s_lshl_b32 s57, s56, 6\n"
    "s_add_u32 s68, s57, 387072\n"
    "s_add_u32 s74, s57, 552960\n"
    "s_and_b32 s57, s49, 3\n"
    "s_mul_i32 s57, s57, 18432\n"
    "s_add_u32 s74, s74, s57\n"
    "s_add_u32 s75, s74, 9216\n"
    "s_mov_b32 s88, -1\n"
    "s_mov_b32 s89, 0\n"
    "s_add_u32 s69, s68, 36864\n"
    "s_add_u32 s70, s68, 73728\n"
    "s_add_u32 s71, s68, 110592\n"
    "s_add_u32 s72, s68, 147456\n"
    "v_mov_b32 v76, 0\n"
    "v_mov_b32 v77, 0\n"
    "v_mov_b32 v78, 0\n"
    "v_mov_b32 v79, 0\n"
    ".Lts_192_%=:\n"
    "s_memrealtime s[90:91]\n"
    "s_waitcnt lgkmcnt(0)\n"
    "s_sub_u32 s90, s90, s63\n"
    "s_cmp_lt_u32 s90, 1397\n"
    "s_cbranch_scc1 .Lts_192_%=\n"
    "s_mov_b32 s60, 65536\n"
    "buffer_load_dwordx4 v[96:99], v57, s[20:23], s74 offen sc1\n"
    "buffer_load_dwordx4 v[100:103], v57, s[20:23], s75 offen sc1\n"
    "s_waitcnt vmcnt(1)\n"
    "v_xor_b32 v96, s92, v96\n"
    "v_xor_b32 v97, s92, v97\n"
    "v_xor_b32 v98, s92, v98\n"
    "v_xor_b32 v99, s92, v99\n"
    "v_or3_b32 v12, v96, v97, v98\n"
    "v_bitop3_b32 v12, v12, v99, s93 bitop3:0xa8\n"
    "v_cmp_ne_u32 s[76:77], 0, v12\n"
    "s_waitcnt vmcnt(0)\n"
    "v_xor_b32 v100, s92, v100\n"
    "v_xor_b32 v101, s92, v101\n"
    "v_xor_b32 v102, s92, v102\n"
    "v_xor_b32 v103, s92, v103\n"
    "v_or3_b32 v12, v100, v101, v102\n"
    "v_bitop3_b32 v12, v12, v103, s93 bitop3:0xa8\n"
    "v_cmp_ne_u32 s[78:79], 0, v12\n"
    "s_nop 0\n"
    "s_or_b64 s[86:87], s[76:77], s[78:79]\n"
    "s_cbranch_scc0 .Lgd_195_%=\n"
    ".Lgp_194_%=:\n"
    "s_mov_b64 exec, s[76:77]\n"
    "s_cbranch_execz .Lgs_196_%=\n"
    "buffer_load_dwordx4 v[96:99], v57, s[20:23], s74 offen sc1\n"
    ".Lgs_196_%=:\n"
    "s_mov_b64 exec, s[78:79]\n"
    "s_cbranch_execz .Lgs_197_%=\n"
    "buffer_load_dwordx4 v[100:103], v57, s[20:23], s75 offen sc1\n"
    ".Lgs_197_%=:\n"
    "s_waitcnt vmcnt(0)\n"
    "s_mov_b64 exec, s[76:77]\n"
    "s_cbranch_execz .Lgs_198_%=\n"
    "v_xor_b32 v96, s92, v96\n"
    "v_xor_b32 v97, s92, v97\n"
    "v_xor_b32 v98, s92, v98\n"
    "v_xor_b32 v99, s92, v99\n"
    "v_or3_b32 v12, v96, v97, v98\n"
    "v_bitop3_b32 v12, v12, v99, s93 bitop3:0xa8\n"
    "v_cmp_ne_u32 s[76:77], 0, v12\n"
    ".Lgs_198_%=:\n"
    "s_mov_b64 exec, s[78:79]\n"
    "s_cbranch_execz .Lgs_199_%=\n"
    "v_xor_b32 v100, s92, v100\n"
    "v_xor_b32 v101, s92, v101\n"
    "v_xor_b32 v102, s92, v102\n"
    "v_xor_b32 v103, s92, v103\n"
    "v_or3_b32 v12, v100, v101, v102\n"
    "v_bitop3_b32 v12, v12, v103, s93 bitop3:0xa8\n"
    "v_cmp_ne_u32 s[78:79], 0, v12\n"
    ".Lgs_199_%=:\n"
    "s_nop 0\n"
    "s_or_b64 s[86:87], s[76:77], s[78:79]\n"
    "s_cbranch_scc0 .Lgd_195_%=\n"
    "s_sub_u32 s60, s60, 1\n"
    "s_cbranch_scc0 .Lgp_194_%=\n"
    ".Lgd_195_%=:\n"
    "s_mov_b64 exec, -1\n"
    "v_pk_add_f32 v[104:105], v[96:97], v[100:101]\n"
    "v_pk_add_f32 v[106:107], v[98:99], v[102:103]\n"
    "s_nop 1\n"
    "v_permlane32_swap_b32 v104, v106\n"
    "v_permlane32_swap_b32 v105, v107\n"
    "v_add_f32 v104, v104, v106\n"
    "v_add_f32 v105, v105, v107\n"
    "s_nop 1\n"
    "v_permlane16_swap_b32 v104, v105\n"
    "v_add_f32 v104, v104, v105\n"
    "v_cvt_pk_bf16_f32 v104, v104, v104\n"
    "v_lshlrev_b32 v104, 16, v104\n"
    "v_mul_f32 v104, v52, v104\n"
    "v_cvt_pk_bf16_f32 v104, v104, v104\n"
    "v_lshlrev_b32 v104, 16, v104\n"
    "v_add_f32 v51, v51, v104\n"
    "v_cvt_pk_bf16_f32 v51, v51, v51\n"
    "v_lshlrev_b32 v51, 16, v51\n"
    "s_sleep 33\n"
    ".Lts_200_%=:\n"
    "s_memrealtime s[90:91]\n"
    "s_waitcnt lgkmcnt(0)\n"
    "s_sub_u32 s90, s90, s63\n"
    "s_cmp_lt_u32 s90, 1543\n"
    "s_cbranch_scc1 .Lts_200_%=\n"
    "s_mov_b32 s60, 65536\n"
    "buffer_load_dwordx4 v[60:63], v56, s[20:23], s68 offen sc1\n"
    "buffer_load_dwordx4 v[64:67], v56, s[20:23], s69 offen sc1\n"
    "buffer_load_dwordx4 v[68:71], v56, s[20:23], s70 offen sc1\n"
    "buffer_load_dwordx4 v[72:75], v56, s[20:23], s71 offen sc1\n"
    "s_mov_b64 exec, s[88:89]\n"
    "buffer_load_dwordx4 v[76:79], v56, s[20:23], s72 offen sc1\n"
    "s_mov_b64 exec, -1\n"
    "s_waitcnt vmcnt(4)\n"
    "v_xor_b32 v60, s92, v60\n"
    "v_xor_b32 v61, s92, v61\n"
    "v_xor_b32 v62, s92, v62\n"
    "v_xor_b32 v63, s92, v63\n"
    "v_or3_b32 v12, v60, v61, v62\n"
    "v_bitop3_b32 v12, v12, v63, s93 bitop3:0xa8\n"
    "v_cmp_ne_u32 s[76:77], 0, v12\n"
    "s_waitcnt vmcnt(3)\n"
    "v_xor_b32 v64, s92, v64\n"
    "v_xor_b32 v65, s92, v65\n"
    "v_xor_b32 v66, s92, v66\n"
    "v_xor_b32 v67, s92, v67\n"
    "v_or3_b32 v12, v64, v65, v66\n"
    "v_bitop3_b32 v12, v12, v67, s93 bitop3:0xa8\n"
    "v_cmp_ne_u32 s[78:79], 0, v12\n"
    "s_waitcnt vmcnt(2)\n"
    "v_xor_b32 v68, s92, v68\n"
    "v_xor_b32 v69, s92, v69\n"
    "v_xor_b32 v70, s92, v70\n"
    "v_xor_b32 v71, s92, v71\n"
    "v_or3_b32 v12, v68, v69, v70\n"
    "v_bitop3_b32 v12, v12, v71, s93 bitop3:0xa8\n"
    "v_cmp_ne_u32 s[80:81], 0, v12\n"
    "s_waitcnt vmcnt(1)\n"
    "v_xor_b32 v72, s92, v72\n"
    "v_xor_b32 v73, s92, v73\n"
    "v_xor_b32 v74, s92, v74\n"
    "v_xor_b32 v75, s92, v75\n"
    "v_or3_b32 v12, v72, v73, v74\n"
    "v_bitop3_b32 v12, v12, v75, s93 bitop3:0xa8\n"
    "v_cmp_ne_u32 s[82:83], 0, v12\n"
    "s_waitcnt vmcnt(0)\n"
    "s_mov_b64 exec, s[88:89]\n"
    "v_xor_b32 v76, s92, v76\n"
    "v_xor_b32 v77, s92, v77\n"
    "v_xor_b32 v78, s92, v78\n"
    "v_xor_b32 v79, s92, v79\n"
    "v_or3_b32 v12, v76, v77, v78\n"
    "v_bitop3_b32 v12, v12, v79, s93 bitop3:0xa8\n"
    "v_cmp_ne_u32 s[84:85], 0, v12\n"
    "s_or_b64 s[86:87], s[76:77], s[78:79]\n"
    "s_or_b64 s[86:87], s[86:87], s[80:81]\n"
    "s_or_b64 s[86:87], s[86:87], s[82:83]\n"
    "s_or_b64 s[86:87], s[86:87], s[84:85]\n"
    "s_cbranch_scc0 .Lgd_203_%=\n"
    ".Lgp_202_%=:\n"
    "s_mov_b64 exec, s[76:77]\n"
    "s_cbranch_execz .Lgs_204_%=\n"
    "buffer_load_dwordx4 v[60:63], v56, s[20:23], s68 offen sc1\n"
    ".Lgs_204_%=:\n"
    "s_mov_b64 exec, s[78:79]\n"
    "s_cbranch_execz .Lgs_205_%=\n"
    "buffer_load_dwordx4 v[64:67], v56, s[20:23], s69 offen sc1\n"
    ".Lgs_205_%=:\n"
    "s_mov_b64 exec, s[80:81]\n"
    "s_cbranch_execz .Lgs_206_%=\n"
    "buffer_load_dwordx4 v[68:71], v56, s[20:23], s70 offen sc1\n"
    ".Lgs_206_%=:\n"
    "s_mov_b64 exec, s[82:83]\n"
    "s_cbranch_execz .Lgs_207_%=\n"
    "buffer_load_dwordx4 v[72:75], v56, s[20:23], s71 offen sc1\n"
    ".Lgs_207_%=:\n"
    "s_mov_b64 exec, s[84:85]\n"
    "s_cbranch_execz .Lgs_208_%=\n"
    "buffer_load_dwordx4 v[76:79], v56, s[20:23], s72 offen sc1\n"
    ".Lgs_208_%=:\n"
    "s_waitcnt vmcnt(0)\n"
    "s_mov_b64 exec, s[76:77]\n"
    "s_cbranch_execz .Lgs_209_%=\n"
    "v_xor_b32 v60, s92, v60\n"
    "v_xor_b32 v61, s92, v61\n"
    "v_xor_b32 v62, s92, v62\n"
    "v_xor_b32 v63, s92, v63\n"
    "v_or3_b32 v12, v60, v61, v62\n"
    "v_bitop3_b32 v12, v12, v63, s93 bitop3:0xa8\n"
    "v_cmp_ne_u32 s[76:77], 0, v12\n"
    ".Lgs_209_%=:\n"
    "s_mov_b64 exec, s[78:79]\n"
    "s_cbranch_execz .Lgs_210_%=\n"
    "v_xor_b32 v64, s92, v64\n"
    "v_xor_b32 v65, s92, v65\n"
    "v_xor_b32 v66, s92, v66\n"
    "v_xor_b32 v67, s92, v67\n"
    "v_or3_b32 v12, v64, v65, v66\n"
    "v_bitop3_b32 v12, v12, v67, s93 bitop3:0xa8\n"
    "v_cmp_ne_u32 s[78:79], 0, v12\n"
    ".Lgs_210_%=:\n"
    "s_mov_b64 exec, s[80:81]\n"
    "s_cbranch_execz .Lgs_211_%=\n"
    "v_xor_b32 v68, s92, v68\n"
    "v_xor_b32 v69, s92, v69\n"
    "v_xor_b32 v70, s92, v70\n"
    "v_xor_b32 v71, s92, v71\n"
    "v_or3_b32 v12, v68, v69, v70\n"
    "v_bitop3_b32 v12, v12, v71, s93 bitop3:0xa8\n"
    "v_cmp_ne_u32 s[80:81], 0, v12\n"
    ".Lgs_211_%=:\n"
    "s_mov_b64 exec, s[82:83]\n"
    "s_cbranch_execz .Lgs_212_%=\n"
    "v_xor_b32 v72, s92, v72\n"
    "v_xor_b32 v73, s92, v73\n"
    "v_xor_b32 v74, s92, v74\n"
    "v_xor_b32 v75, s92, v75\n"
    "v_or3_b32 v12, v72, v73, v74\n"
    "v_bitop3_b32 v12, v12, v75, s93 bitop3:0xa8\n"
    "v_cmp_ne_u32 s[82:83], 0, v12\n"
    ".Lgs_212_%=:\n"
    "s_mov_b64 exec, s[84:85]\n"
    "s_cbranch_execz .Lgs_213_%=\n"
    "v_xor_b32 v76, s92, v76\n"
    "v_xor_b32 v77, s92, v77\n"
    "v_xor_b32 v78, s92, v78\n"
    "v_xor_b32 v79, s92, v79\n"
    "v_or3_b32 v12, v76, v77, v78\n"
    "v_bitop3_b32 v12, v12, v79, s93 bitop3:0xa8\n"
    "v_cmp_ne_u32 s[84:85], 0, v12\n"
    ".Lgs_213_%=:\n"
    "s_nop 0\n"
    "s_or_b64 s[86:87], s[76:77], s[78:79]\n"
    "s_or_b64 s[86:87], s[86:87], s[80:81]\n"
    "s_or_b64 s[86:87], s[86:87], s[82:83]\n"
    "s_or_b64 s[86:87], s[86:87], s[84:85]\n"
    "s_cbranch_scc0 .Lgd_203_%=\n"
    "s_sub_u32 s60, s60, 1\n"
    "s_cbranch_scc0 .Lgp_202_%=\n"
    ".Lgd_203_%=:\n"
    "s_mov_b64 exec, -1\n"
    "v_pk_add_f32 v[100:101], v[76:77], v[60:61]\n"
    "v_pk_add_f32 v[102:103], v[78:79], v[62:63]\n"
    "v_pk_add_f32 v[104:105], v[64:65], v[68:69]\n"
    "v_pk_add_f32 v[106:107], v[66:67], v[70:71]\n"
    "v_pk_add_f32 v[100:101], v[100:101], v[72:73]\n"
    "v_pk_add_f32 v[102:103], v[102:103], v[74:75]\n"
    "v_pk_add_f32 v[100:101], v[100:101], v[104:105]\n"
    "v_pk_add_f32 v[102:103], v[102:103], v[106:107]\n"
    "s_mov_b32 s88, 0x000f000f\n"
    "s_mov_b32 s89, 0x000f000f\n"
    "v_permlane32_swap_b32 v100, v102\n"
    "v_permlane32_swap_b32 v101, v103\n"
    "v_add_f32 v100, v100, v102\n"
    "v_add_f32 v101, v101, v103\n"
    "s_nop 1\n"
    "v_permlane16_swap_b32 v100, v101\n"
    "v_add_f32 v100, v100, v101\n"
    "s_nop 1\n"
    "v_add_f32_dpp v100, v100, v100 row_ror:4 row_mask:0xf bank_mask:0xf bound_ctrl:1\n"
    "s_nop 1\n"
    "v_add_f32_dpp v100, v100, v100 row_ror:8 row_mask:0xf bank_mask:0xf bound_ctrl:1\n"
    "v_cvt_pk_bf16_f32 v100, v100, v100\n"
    "v_lshlrev_b32 v100, 16, v100\n"
    "v_mul_f32 v100, v54, v100\n"
    "v_cvt_pk_bf16_f32 v100, v100, v100\n"
    "v_lshlrev_b32 v100, 16, v100\n"
    "v_add_f32 v100, v51, v100\n"
    "v_cvt_pk_bf16_f32 v100, v100, v100\n"
    "s_mov_b64 exec, s[88:89]\n"
    "buffer_store_short v100, v49, s[32:35], 0 offen\n"
    "s_mov_b64 exec, 1\n"
    "v_mov_b32 v12, s54\n"
    "s_cmp_lg_u32 s56, 0\n"
    "s_cbranch_scc1 .Lnoep_%=\n"
    "buffer_store_dword v12, off, s[20:23], 0 sc1\n"
    ".Lnoep_%=:\n"
    "s_mov_b64 exec, -1\n"
    "s_endpgm\n"
    ".LH_11_%=:\n"
    "s_cmp_gt_u32 s48, 2\n"
    "s_cbranch_scc1 .LHw_214_%=\n"
    "buffer_load_dword v124, off, s[20:23], 0 sc1\n"
    "v_lshlrev_b32 v127, 2, v1\n"
    "s_sub_u32 s56, s2, 244\n"
    "s_mul_i32 s56, s56, 3\n"
    "s_add_u32 s56, s56, s48\n"
    "buffer_load_dword v128, v127, s[28:31], 0 offen\n"
    "buffer_load_dword v129, v127, s[28:31], 0 offen offset:256\n"
    "buffer_load_dword v130, v127, s[28:31], 0 offen offset:512\n"
    "buffer_load_dword v131, v127, s[28:31], 0 offen offset:768\n"
    "buffer_load_dword v132, v127, s[28:31], 0 offen offset:1024\n"
    "s_lshl_b32 s75, s56, 7\n"
    "v_lshlrev_b32 v12, 1, v1\n"
    "buffer_load_ushort v133, v12, s[28:31], s75 offen\n"
    "s_waitcnt vmcnt(0)\n"
    "s_memrealtime s[90:91]\n"
    "s_waitcnt lgkmcnt(0)\n"
    "s_sub_u32 s90, s90, 65\n"
    "s_mov_b32 s62, s90\n"
    "s_waitcnt vmcnt(0) lgkmcnt(0)\n"
    "v_readfirstlane_b32 s54, v124\n"
    "s_nop 0\n"
    "s_add_u32 s54, s54, 1\n"
    "s_cmp_eq_u32 s54, 0\n"
    "s_cselect_b32 s54, 1, s54\n"
    "s_cmp_lg_u32 s48, 0\n"
    "s_cbranch_scc1 .Lfd_215_%=\n"
    "s_memrealtime s[90:91]\n"
    "v_mov_b32 v164, 0\n"
    "s_waitcnt lgkmcnt(0)\n"
    "s_sub_u32 s90, s90, 65\n"
    "s_or_b32 s90, s90, 1\n"
    "v_mov_b32 v165, s90\n"
    "ds_write2_b32 v164, v165, v165 offset0:170 offset1:173\n"
    ".Lfd_215_%=:\n"
    "v_lshlrev_b32 v144, 16, v128\n"
    "v_and_b32 v145, 0xffff0000, v128\n"
    "v_lshlrev_b32 v146, 16, v129\n"
    "v_and_b32 v147, 0xffff0000, v129\n"
    "v_lshlrev_b32 v148, 16, v130\n"
    "v_and_b32 v149, 0xffff0000, v130\n"
    "v_lshlrev_b32 v150, 16, v131\n"
    "v_and_b32 v151, 0xffff0000, v131\n"
    "v_lshlrev_b32 v152, 16, v132\n"
    "v_and_b32 v153, 0xffff0000, v132\n"
    "v_add_f32 v154, v144, v145\n"
    "v_add_f32 v154, v154, v146\n"
    "v_add_f32 v154, v154, v147\n"
    "v_add_f32 v154, v154, v148\n"
    "v_add_f32 v154, v154, v149\n"
    "v_add_f32 v154, v154, v150\n"
    "v_add_f32 v154, v154, v151\n"
    "v_add_f32 v154, v154, v152\n"
    "v_add_f32 v154, v154, v153\n"
    "s_nop 1\n"
    "v_add_f32_dpp v154, v154, v154 quad_perm:[1,0,3,2] row_mask:0xf bank_mask:0xf bound_ctrl:1\n"
    "s_nop 1\n"
    "v_add_f32_dpp v154, v154, v154 quad_perm:[2,3,0,1] row_mask:0xf bank_mask:0xf bound_ctrl:1\n"
    "s_nop 1\n"
    "v_add_f32_dpp v154, v154, v154 row_half_mirror row_mask:0xf bank_mask:0xf bound_ctrl:1\n"
    "s_nop 1\n"
    "v_add_f32_dpp v154, v154, v154 row_ror:8 row_mask:0xf bank_mask:0xf bound_ctrl:1\n"
    "v_mov_b32 v155, v154\n"
    "s_nop 1\n"
    "v_permlane16_swap_b32 v154, v155\n"
    "v_add_f32 v154, v154, v155\n"
    "v_mov_b32 v155, v154\n"
    "s_nop 1\n"
    "v_permlane32_swap_b32 v154, v155\n"
    "v_add_f32 v154, v154, v155\n"
    "v_mul_f32 v154, 0x3ae38e39, v154\n"
    "v_sub_f32 v144, v144, v154\n"
    "v_sub_f32 v145, v145, v154\n"
    "v_sub_f32 v146, v146, v154\n"
    "v_sub_f32 v147, v147, v154\n"
    "v_sub_f32 v148, v148, v154\n"
    "v_sub_f32 v149, v149, v154\n"
    "v_sub_f32 v150, v150, v154\n"
    "v_sub_f32 v151, v151, v154\n"
    "v_sub_f32 v152, v152, v154\n"
    "v_sub_f32 v153, v153, v154\n"
    "v_mul_f32 v155, v152, v152\n"
    "v_fmac_f32 v155, v153, v153\n"
    "v_cmp_gt_u32 vcc, 32, v1\n"
    "s_nop 1\n"
    "v_cndmask_b32 v155, 0, v155, vcc\n"
    "v_fmac_f32 v155, v144, v144\n"
    "v_fmac_f32 v155, v145, v145\n"
    "v_fmac_f32 v155, v146, v146\n"
    "v_fmac_f32 v155, v147, v147\n"
    "v_fmac_f32 v155, v148, v148\n"
    "v_fmac_f32 v155, v149, v149\n"
    "v_fmac_f32 v155, v150, v150\n"
    "v_fmac_f32 v155, v151, v151\n"
    "s_nop 1\n"
    "v_add_f32_dpp v155, v155, v155 quad_perm:[1,0,3,2] row_mask:0xf bank_mask:0xf bound_ctrl:1\n"
    "s_nop 1\n"
    "v_add_f32_dpp v155, v155, v155 quad_perm:[2,3,0,1] row_mask:0xf bank_mask:0xf bound_ctrl:1\n"
    "s_nop 1\n"
    "v_add_f32_dpp v155, v155, v155 row_half_mirror row_mask:0xf bank_mask:0xf bound_ctrl:1\n"
    "s_nop 1\n"
    "v_add_f32_dpp v155, v155, v155 row_ror:8 row_mask:0xf bank_mask:0xf bound_ctrl:1\n"
    "v_mov_b32 v156, v155\n"
    "s_nop 1\n"
    "v_permlane16_swap_b32 v155, v156\n"
    "v_add_f32 v155, v155, v156\n"
    "v_mov_b32 v156, v155\n"
    "s_nop 1\n"
    "v_permlane32_swap_b32 v155, v156\n"
    "v_add_f32 v155, v155, v156\n"
    "v_mul_f32 v155, 0x3ae38e39, v155\n"
    "v_cvt_pk_bf16_f32 v156, v154, v155\n"
    "v_lshlrev_b32 v154, 16, v156\n"
    "v_and_b32 v155, 0xffff0000, v156\n"
    "v_add_f32 v155, 0x3727c5ac, v155\n"
    "v_cvt_pk_bf16_f32 v155, v155, v155\n"
    "v_lshlrev_b32 v155, 16, v155\n"
    "v_sqrt_f32 v155, v155\n"
    "s_nop 0\n"
    "v_cvt_pk_bf16_f32 v155, v155, v155\n"
    "v_lshlrev_b32 v155, 16, v155\n"
    "v_rcp_f32 v156, v155\n"
    "v_lshlrev_b32 v144, 16, v133\n"
    "v_sub_f32 v144, v144, v154\n"
    "v_cvt_pk_bf16_f32 v144, v144, v144\n"
    "v_lshlrev_b32 v144, 16, v144\n"
    "v_mul_f32 v13, v144, v156\n"
    "v_fma_f32 v144, -v13, v155, v144\n"
    "v_fma_f32 v144, v144, v156, v13\n"
    "v_cvt_pk_bf16_f32 v144, v144, v144\n"
    "v_lshlrev_b32 v144, 16, v144\n"
    "s_lshl_b32 s76, s56, 9\n"
    "s_add_u32 s76, s76, 8192\n"
    "s_lshl_b32 s77, s56, 8\n"
    "s_add_u32 s77, s77, 45056\n"
    ".Ltw_216_%=:\n"
    "s_memrealtime s[90:91]\n"
    "s_waitcnt lgkmcnt(0)\n"
    "s_sub_u32 s90, s90, s62\n"
    "s_cmp_ge_u32 s90, 250\n"
    "s_cbranch_scc1 .Ltwd_217_%=\n"
    "s_sleep 1\n"
    "s_branch .Ltw_216_%=\n"
    ".Ltwd_217_%=:\n"
    "s_mov_b32 s60, 65536\n"
    ".Ltp_218_%=:\n"
    "buffer_load_dwordx2 v[88:89], v125, s[20:23], s76 offen sc1\n"
    "s_add_u32 s57, s76, 9216\n"
    "buffer_load_dwordx2 v[90:91], v125, s[20:23], s57 offen sc1\n"
    "s_add_u32 s57, s76, 18432\n"
    "buffer_load_dwordx2 v[92:93], v125, s[20:23], s57 offen sc1\n"
    "s_add_u32 s57, s76, 27648\n"
    "buffer_load_dwordx2 v[94:95], v125, s[20:23], s57 offen sc1\n"
    "s_add_u32 s57, s76, 4608\n"
    "buffer_load_dwordx2 v[96:97], v125, s[20:23], s57 offen sc1\n"
    "s_add_u32 s57, s76, 13824\n"
    "buffer_load_dwordx2 v[98:99], v125, s[20:23], s57 offen sc1\n"
    "s_add_u32 s57, s76, 23040\n"
    "buffer_load_dwordx2 v[100:101], v125, s[20:23], s57 offen sc1\n"
    "s_add_u32 s57, s76, 32256\n"
    "buffer_load_dwordx2 v[102:103], v125, s[20:23], s57 offen sc1\n"
    "s_waitcnt vmcnt(0)\n"
    "v_xor_b32 v12, s54, v89\n"
    "v_xor_b32 v13, s54, v91\n"
    "v_or_b32 v12, v12, v13\n"
    "v_xor_b32 v13, s54, v93\n"
    "v_or_b32 v12, v12, v13\n"
    "v_xor_b32 v13, s54, v95\n"
    "v_or_b32 v12, v12, v13\n"
    "v_xor_b32 v13, s54, v97\n"
    "v_or_b32 v12, v12, v13\n"
    "v_xor_b32 v13, s54, v99\n"
    "v_or_b32 v12, v12, v13\n"
    "v_xor_b32 v13, s54, v101\n"
    "v_or_b32 v12, v12, v13\n"
    "v_xor_b32 v13, s54, v103\n"
    "v_or_b32 v12, v12, v13\n"
    "v_cmp_ne_u32 vcc, 0, v12\n"
    "s_cbranch_vccz .Ltpd_219_%=\n"
    "s_sleep 0\n"
    "s_sub_u32 s60, s60, 1\n"
    "s_cbranch_scc0 .Ltp_218_%=\n"
    ".Ltpd_219_%=:\n"
    "v_add_f32 v88, v88, v90\n"
    "v_add_f32 v92, v92, v94\n"
    "v_add_f32 v96, v96, v98\n"
    "v_add_f32 v100, v100, v102\n"
    "v_add_f32 v88, v88, v92\n"
    "v_add_f32 v96, v96, v100\n"
    "v_cvt_pk_bf16_f32 v12, v88, v96\n"
    "v_lshlrev_b32 v88, 16, v12\n"
    "v_and_b32 v96, 0xffff0000, v12\n"
    "v_add_f32 v96, 1.0, v96\n"
    "v_cvt_pk_bf16_f32 v96, v96, v96\n"
    "v_lshlrev_b32 v96, 16, v96\n"
    "v_mul_f32 v144, v144, v96\n"
    "v_cvt_pk_bf16_f32 v144, v144, v144\n"
    "v_lshlrev_b32 v144, 16, v144\n"
    "v_add_f32 v144, v144, v88\n"
    "s_nop 1\n"
    "v_mov_b32_dpp v145, v144 quad_perm:[1,0,3,2] row_mask:0xf bank_mask:0xf bound_ctrl:1\n"
    "v_cvt_pk_bf16_f32 v88, v144, v145\n"
    "v_mov_b32 v89, s54\n"
    "v_mov_b32 v91, s54\n"
    "v_mov_b32_dpp v90, v88 quad_perm:[2,3,0,1] row_mask:0xf bank_mask:0xf bound_ctrl:1\n"
    "s_mov_b32 s78, 0x11111111\n"
    "s_mov_b32 s79, 0x11111111\n"
    "s_mov_b64 exec, s[78:79]\n"
    "buffer_store_dwordx4 v[88:91], v127, s[20:23], s77 offen sc1\n"
    "s_mov_b64 exec, -1\n"
    ".LHw_214_%=:\n"
    "s_barrier\n"
    "s_branch .LHb_12_%=\n"
    ".LEND_%=:\n"
    :: "{s[0:1]}"(__builtin_amdgcn_kernarg_segment_ptr()), "{s2}"(blockIdx.x), "{v0}"(threadIdx.x), "{s3}"((unsigned)(unsigned long long)lds)
    : "memory", "vcc", "scc", "s4", "s5", "s6", "s7", "s8", "s9", "s10", "s11", "s12", "s13", "s14", "s15", "s16", "s17", "s18", "s19", "s20", "s21", "s22", "s23", "s24", "s25", "s26", "s27", "s28", "s29", "s30", "s31", "s33", "s34", "s35", "s36", "s37", "s38", "s39", "s40", "s41", "s42", "s43", "s44", "s45", "s46", "s47", "s48", "s49", "s50", "s51", "s52", "s53", "s54", "s55", "s56", "s57", "s58", "s59", "s60", "s61", "s62", "s63", "s64", "s65", "s66", "s67", "s68", "s69", "s70", "s71", "s72", "s73", "s74", "s75", "s76", "s77", "s78", "s79", "s80", "s81", "s82", "s83", "s84", "s85", "s86", "s87", "s88", "s89", "s90", "s91", "s92", "s93", "s94", "s95", "v1", "v2", "v3", "v4", "v5", "v6", "v7", "v8", "v9", "v10", "v11", "v12", "v13", "v14", "v15", "v16", "v17", "v18", "v19", "v20", "v21", "v22", "v23", "v24", "v25", "v26", "v27", "v28", "v29", "v30", "v31", "v32", "v33", "v34", "v35", "v36", "v37", "v38", "v39", "v40", "v41", "v42", "v43", "v44", "v45", "v46", "v47", "v48", "v49", "v50", "v51", "v52", "v53", "v54", "v55", "v56", "v57", "v58", "v59", "v60", "v61", "v62", "v63", "v64", "v65", "v66", "v67", "v68", "v69", "v70", "v71", "v72", "v73", "v74", "v75", "v76", "v77", "v78", "v79", "v80", "v81", "v82", "v83", "v84", "v85", "v86", "v87", "v88", "v89", "v90", "v91", "v92", "v93", "v94", "v95", "v96", "v97", "v98", "v99", "v100", "v101", "v102", "v103", "v104", "v105", "v106", "v107", "v108", "v109", "v110", "v111", "v112", "v113", "v114", "v115", "v116", "v117", "v118", "v119", "v120", "v121", "v122", "v123", "v124", "v125", "v126", "v127", "v128", "v129", "v130", "v131", "v132", "v133", "v134", "v135", "v136", "v137", "v138", "v139", "v140", "v141", "v142", "v143", "v144", "v145", "v146", "v147", "v148", "v149", "v150", "v151", "v152", "v153", "v154", "v155", "v156", "v157", "v158", "v159", "v160", "v161", "v162", "v163", "v164", "v165", "v166", "v167");
}

extern "C" void kernel_launch(void* const* d_in, const int* in_sizes, int n_in, void* d_out, int out_size, void* d_ws, size_t ws_size,
                              hipStream_t stream) {
  fused_k<<<256, 576, 0, stream>>>(d_in[9], 3981312u, 0x20000u, d_in[8], 1152u, 0x20000u, d_in[1], 33554432u, 0x20000u, d_in[2], 33554432u, 0x20000u, d_ws, 2097152u, 0x20000u, d_in[3], 4718592u, 0x20000u, d_in[0], 1152u, 0x20000u, d_out, 1152u, 0x20000u, d_in[4], 4718592u, 0x20000u, d_in[6], 5308416u, 0x20000u, d_in[7], 5308416u, 0x20000u);
}
```
